```python
import torch

def _softmax(x, axis=-1):
    return torch.softmax(x, dim=axis)
def _gelu(x):
    return torch.nn.functional.gelu(x, approximate="tanh")
def _silu(x):
    return torch.nn.functional.silu(x)
def _sigmoid(x):
    return torch.sigmoid(x)
def _layernorm(x, axis=-1, eps=1e-5):
    m = x.mean(dim=axis, keepdim=True)
    v = x.var(dim=axis, keepdim=True, unbiased=False)
    return (x - m) / torch.sqrt(v + eps)
def _scatter_add(tgt, idx, src, axis=0):
    return tgt.clone().index_add_(axis, idx, src)
def _segment_sum(src, seg, n):
    out = torch.zeros((n,) + src.shape[1:], dtype=src.dtype)
    return out.index_add_(0, seg, src)
def _pad(x, pw):
    flat = tuple(v for pair in reversed(pw) for v in pair)
    return torch.nn.functional.pad(x, flat)

kernel_name = 'sequential_preLN__compressed_kv_cla__swiglu_ffn__decode'

def setup_inputs():

    _w = (lambda a: (lambda t: t.to(torch.bfloat16) if t.is_floating_point() else t)(torch.as_tensor(a)))
    def _A_setup():
        B, H, Sq, Sk, D, M = 4, 32, 1, 4096, 128, 512
        Dc = 4 * D
        return {
            "x": _w((lambda *s: torch.randn(*s, dtype=torch.bfloat16) * 0.05)(B, Sq, M)),
            "c_kv": _w((lambda *s: torch.randn(*s, dtype=torch.bfloat16) * 0.4)(B, Sk, Dc)),
            "w_q": _w((lambda *s: torch.randn(*s, dtype=torch.bfloat16) * 3.2)(M, H, D) * (M ** -0.5)),
            "w_uk": _w((lambda *s: torch.randn(*s, dtype=torch.bfloat16) * 3.2)(Dc, H, D) * (Dc ** -0.5)),
            "w_uv": _w((lambda *s: torch.randn(*s, dtype=torch.bfloat16) * 0.4)(Dc, H, D) * (Dc ** -0.5)),
            "w_o": _w((lambda *s: torch.randn(*s, dtype=torch.bfloat16) * 0.4)(H, D, M) * ((H * D) ** -0.5)),
        }
    _A_in = _A_setup()

    def _B_setup():
        B, S, D, F = 4, 1, 512, 1376
        return {
            "x": _w((lambda *s: torch.randn(*s, dtype=torch.bfloat16) * 0.05)(B, S, D)),
            "w_gate": _w((lambda *s: torch.randn(*s, dtype=torch.bfloat16) * 0.05)(D, F)),
            "w_up": _w((lambda *s: torch.randn(*s, dtype=torch.bfloat16) * 0.05)(D, F)),
            "w_down": _w((lambda *s: torch.randn(*s, dtype=torch.bfloat16) * 0.05)(F, D)),
        }
    _B_in = _B_setup()

    return {
        "A_x": _A_in["x"],
        "A_c_kv": _A_in["c_kv"],
        "A_w_q": _A_in["w_q"],
        "A_w_uk": _A_in["w_uk"],
        "A_w_uv": _A_in["w_uv"],
        "A_w_o": _A_in["w_o"],
        "B_x": _B_in["x"],
        "B_w_gate": _B_in["w_gate"],
        "B_w_up": _B_in["w_up"],
        "B_w_down": _B_in["w_down"],
    }

def reference(A_x, A_c_kv, A_w_q, A_w_uk, A_w_uv, A_w_o, B_x, B_w_gate, B_w_up, B_w_down):
    def _A_ref(x, c_kv, w_q, w_uk, w_uv, w_o):
        B, Sq, M = x.shape[0], x.shape[1], x.shape[2]
        Sk = c_kv.shape[1]
        H, D = w_q.shape[1], w_q.shape[2]
        q = torch.einsum("bqm,mhd->bhqd", x, w_q)
        k = torch.einsum("bkc,chd->bhkd", c_kv, w_uk)
        v = torch.einsum("bkc,chd->bhkd", c_kv, w_uv)
        scores = torch.einsum("bhqd,bhkd->bhqk", q, k) * (D ** -0.5)
        off = Sk - Sq
        qi = torch.arange(Sq)[:, None]
        kj = torch.arange(Sk)[None, :]
        cmask = kj <= (qi + off)
        scores = torch.where(cmask[None, None], scores, -1e9)
        p = _softmax(scores, axis=-1)
        ctx = torch.einsum("bhqk,bhkd->bhqd", p, v)
        return torch.einsum("bhqd,hdm->bqm", ctx, w_o)

    def _B_ref(x, w_gate, w_up, w_down):
        g = _silu(x @ w_gate)
        u = x @ w_up
        return (g * u) @ w_down

    x = A_x
    h = x + _A_ref(_layernorm(x, axis=-1), A_c_kv, A_w_q, A_w_uk, A_w_uv, A_w_o)
    h2 = h + _B_ref(_layernorm(h, axis=-1), B_w_gate, B_w_up, B_w_down)
    return h2
```

```cpp
#include <hip/hip_runtime.h>
#include <hip/hip_bf16.h>
#include <stdint.h>

typedef unsigned short u16;
__device__ __forceinline__ float bf2f(u16 b) { return __uint_as_float(((unsigned)b) << 16); }
__device__ __forceinline__ u16 f2bf(float f) { unsigned u = __float_as_uint(f); u += 0x7FFFu + ((u >> 16) & 1u); return (u16)(u >> 16); }

constexpr int B = 4, H = 32, SK = 4096, D = 128, M = 512, DC = 512, F = 1376;
constexpr int NSPLIT = 64, KS = SK / NSPLIT;

__device__ float block_sum(float v, float* red) {
    for (int o = 32; o >= 1; o >>= 1) v += __shfl_xor(v, o);
    int w = threadIdx.x >> 6;
    __syncthreads();
    if ((threadIdx.x & 63) == 0) red[w] = v;
    __syncthreads();
    float s = 0;
    for (int i = 0; i < (int)(blockDim.x >> 6); ++i) s += red[i];
    return s;
}

__global__ void __launch_bounds__(512) k1(const u16* x, const u16* wq, const u16* wuk, float* qabs) {
    __shared__ float xn[B][M]; __shared__ float q[B][D]; __shared__ float red[8];
    int h = blockIdx.x, t = threadIdx.x;
    for (int b = 0; b < B; ++b) {
        float v = bf2f(x[b * M + t]);
        float mean = block_sum(v, red) / M;
        float dv = v - mean;
        float var = block_sum(dv * dv, red) / M;
        xn[b][t] = dv / sqrtf(var + 1e-5f);
    }
    __syncthreads();
    { int b = t >> 7, d = t & 127; float s = 0;
      for (int m = 0; m < M; ++m) s += xn[b][m] * bf2f(wq[(size_t)m * H * D + h * D + d]);
      q[b][d] = s; }
    __syncthreads();
    { int c = t; float s[B] = {0, 0, 0, 0};
      for (int d = 0; d < D; ++d) { float w = bf2f(wuk[(size_t)c * H * D + h * D + d]); for (int b = 0; b < B; ++b) s[b] += q[b][d] * w; }
      for (int b = 0; b < B; ++b) qabs[(b * H + h) * DC + c] = s[b] * 0.08838834764831845f; }
}
__global__ void __launch_bounds__(512) k2(const u16* ckv, const float* qabs, float* opart, float* ml) {
    __shared__ float s[H][KS];
    int b = blockIdx.x / NSPLIT, sp = blockIdx.x % NSPLIT, t = threadIdx.x;
    const u16* kv = ckv + ((size_t)b * SK + sp * KS) * DC;
    for (int i = t; i < H * KS; i += 512) { int h = i / KS, k = i % KS; float a = 0;
        for (int c = 0; c < DC; ++c) a += qabs[(b * H + h) * DC + c] * bf2f(kv[k * DC + c]);
        s[h][k] = a; }
    __syncthreads();
    if (t < H) { float m = -1e30f; for (int k = 0; k < KS; ++k) m = fmaxf(m, s[t][k]); float l = 0;
        for (int k = 0; k < KS; ++k) { float p = __expf(s[t][k] - m); s[t][k] = p; l += p; }
        ml[((b * NSPLIT + sp) * H + t) * 2] = m; ml[((b * NSPLIT + sp) * H + t) * 2 + 1] = l; }
    __syncthreads();
    _Pragma("unroll 1") for (int h = 0; h < H; ++h) { float a = 0; _Pragma("unroll 8") for (int k = 0; k < KS; ++k) a += s[h][k] * bf2f(kv[k * DC + t]);
        opart[((size_t)(b * NSPLIT + sp) * H + h) * DC + t] = a; }
}
__global__ void __launch_bounds__(512) k3(const float* opart, const float* ml, const u16* wuv, float* ctx) {
    __shared__ float o[DC]; __shared__ float part[4][D];
    int b = blockIdx.x / H, h = blockIdx.x % H, t = threadIdx.x;
    float m = -1e30f; for (int sp = 0; sp < NSPLIT; ++sp) m = fmaxf(m, ml[((b * NSPLIT + sp) * H + h) * 2]);
    float L = 0, a = 0;
    for (int sp = 0; sp < NSPLIT; ++sp) { float w = __expf(ml[((b * NSPLIT + sp) * H + h) * 2] - m); L += w * ml[((b * NSPLIT + sp) * H + h) * 2 + 1];
        a += w * opart[((size_t)(b * NSPLIT + sp) * H + h) * DC + t]; }
    o[t] = a / L;
    __syncthreads();
    { int g = t >> 7, d = t & 127; float s = 0; for (int c = g * 128; c < g * 128 + 128; ++c) s += o[c] * bf2f(wuv[(size_t)c * H * D + h * D + d]); part[g][d] = s; }
    __syncthreads();
    if (t < D) ctx[(b * H + h) * D + t] = part[0][t] + part[1][t] + part[2][t] + part[3][t];
}
__global__ void __launch_bounds__(512) k4(const float* ctx, const u16* wo, float* apart) {
    int h = blockIdx.x, t = threadIdx.x; float s[B] = {0, 0, 0, 0};
    for (int d = 0; d < D; ++d) { float w = bf2f(wo[(size_t)(h * D + d) * M + t]); for (int b = 0; b < B; ++b) s[b] += ctx[(b * H + h) * D + d] * w; }
    for (int b = 0; b < B; ++b) apart[(h * B + b) * M + t] = s[b];
}
__global__ void __launch_bounds__(512) k5(const u16* x, const float* apart, const u16* wg, const u16* wu, float* hbuf, float* act) {
    __shared__ float hn[B][M]; __shared__ float red[8]; __shared__ float pg[16][B][32], pu[16][B][32];
    int t = threadIdx.x;
    for (int b = 0; b < B; ++b) {
        float v = bf2f(x[b * M + t]); float a = 0; for (int h = 0; h < H; ++h) a += apart[(h * B + b) * M + t];
        v += a; if (blockIdx.x == 0) hbuf[b * M + t] = v;
        float mean = block_sum(v, red) / M; float dv = v - mean; float var = block_sum(dv * dv, red) / M;
        hn[b][t] = dv / sqrtf(var + 1e-5f);
    }
    __syncthreads();
    int f = blockIdx.x * 32 + (t & 31), g = t >> 5; float sg[B] = {0,0,0,0}, su[B] = {0,0,0,0};
    for (int m = g * 32; m < g * 32 + 32; ++m) { float a = bf2f(wg[(size_t)m * F + f]), c = bf2f(wu[(size_t)m * F + f]);
        for (int b = 0; b < B; ++b) { sg[b] += hn[b][m] * a; su[b] += hn[b][m] * c; } }
    for (int b = 0; b < B; ++b) { pg[g][b][t & 31] = sg[b]; pu[g][b][t & 31] = su[b]; }
    __syncthreads();
    if (t < 128) { int b = t >> 5, j = t & 31; float a = 0, c = 0; for (int i = 0; i < 16; ++i) { a += pg[i][b][j]; c += pu[i][b][j]; }
        act[b * F + blockIdx.x * 32 + j] = a / (1.f + __expf(-a)) * c; }
}
__global__ void __launch_bounds__(512) k6(const float* hbuf, const float* act, const u16* wd, u16* out) {
    __shared__ float part[8][B][64];
    int t = threadIdx.x, m = blockIdx.x * 64 + (t & 63), g = t >> 6; float s[B] = {0,0,0,0};
    for (int f = g; f < F; f += 8) { float w = bf2f(wd[(size_t)f * M + m]); for (int b = 0; b < B; ++b) s[b] += act[b * F + f] * w; }
    for (int b = 0; b < B; ++b) part[g][b][t & 63] = s[b];
    __syncthreads();
    if (t < 256) { int b = t >> 6, j = t & 63; float a = 0; for (int i = 0; i < 8; ++i) a += part[i][b][j];
        out[b * M + blockIdx.x * 64 + j] = f2bf(hbuf[b * M + blockIdx.x * 64 + j] + a); }
}

extern "C" void kernel_launch(void* const* d_in, const int* in_sizes, int n_in, void* d_out, int out_size, void* d_ws, size_t ws_size, hipStream_t stream) {
    const u16* x = (const u16*)d_in[0]; const u16* ckv = (const u16*)d_in[1]; const u16* wq = (const u16*)d_in[2]; const u16* wuk = (const u16*)d_in[3];
    const u16* wuv = (const u16*)d_in[4]; const u16* wo = (const u16*)d_in[5]; const u16* wg = (const u16*)d_in[7]; const u16* wu = (const u16*)d_in[8]; const u16* wd = (const u16*)d_in[9];
    float* ws = (float*)d_ws;
    float* qabs = ws; float* opart = qabs + B * H * DC; float* ml = opart + (size_t)B * NSPLIT * H * DC; float* ctx = ml + B * NSPLIT * H * 2;
    float* apart = ctx + B * H * D; float* hbuf = apart + H * B * M; float* act = hbuf + B * M;
    k1<<<H, 512, 0, stream>>>(x, wq, wuk, qabs);
    k2<<<B * NSPLIT, 512, 0, stream>>>(ckv, qabs, opart, ml);
    k3<<<B * H, 512, 0, stream>>>(opart, ml, wuv, ctx);
    k4<<<H, 512, 0, stream>>>(ctx, wo, apart);
    k5<<<F / 32, 512, 0, stream>>>(x, apart, wg, wu, hbuf, act);
    k6<<<M / 64, 512, 0, stream>>>(hbuf, act, wd, (u16*)d_out);
}
```

```cpp
#include <hip/hip_runtime.h>
#include <stdint.h>

#define GAS __attribute__((address_space(1)))
#define LAS __attribute__((address_space(3)))
typedef unsigned v4u __attribute__((ext_vector_type(4)));
typedef unsigned v2u __attribute__((ext_vector_type(2)));
typedef float f32x4 __attribute__((ext_vector_type(4)));
typedef float f32x2 __attribute__((ext_vector_type(2)));
typedef short s16x4 __attribute__((ext_vector_type(4)));
typedef short s16x8 __attribute__((ext_vector_type(8)));
typedef __bf16 bf16x2 __attribute__((ext_vector_type(2)));
typedef __bf16 bf16x8 __attribute__((ext_vector_type(8)));
typedef unsigned short u16;
typedef unsigned long long u64;
typedef __amdgpu_buffer_rsrc_t rsrc_t;
#define DI __device__ __forceinline__

namespace {
constexpr int NFF = 43;
constexpr int WS_EP = 0;
constexpr int WS_Q1 = 4096;
constexpr int WS_QA = WS_Q1 + 524288;
constexpr int WS_ST = WS_QA + 131072;
constexpr int WS_X1 = WS_ST + 32768;
constexpr int WS_X2 = WS_X1 + 131072;
constexpr int WS_HD = WS_X2 + 32768;
constexpr int WS_O = WS_HD + 64 * 48 * 128;
constexpr int WS_END = WS_O + (4 << 20);
constexpr int KVS = 1040;
constexpr int L_KV = 0, L_Q = 128 * KVS, L_P = 144 * KVS;
constexpr int L_EP = L_P + 4096, L_XN = L_EP + 128, L_SM = L_XN + 4 * 144, L_SS = L_SM + 512, L_FL = L_SS + 512, LDS_BYTES = L_FL + 64;
constexpr int L_OB = L_Q, L_WT = L_Q + 2048, L_RED = L_Q + 4096, L_CS = L_Q + 4096 + 8192, L_STAT = L_CS + 2304, L_ACT = L_STAT + 256;
static_assert(L_ACT + 4 * 80 <= L_P, "tail scratch must fit the q rows");
static_assert(LDS_BYTES <= 163840, "lds");
constexpr unsigned TM = 0x00010001u;
constexpr int SPIN = 1 << 14;
constexpr int OOB = 0x7ffffff0;
#define CODE_BYTES 14336
#define KV_PRE 8
#define Q1_DELAY 12
#define QA_DELAY 16
#define KV_THR 8

struct Params {
    const u16 *x, *ckv, *wq, *wuk, *wuv, *wo, *wg, *wu, *wd;
    unsigned char* ws;
    u16* out;
};

DI v4u ld16(rsrc_t rs, int off) { return __builtin_amdgcn_raw_buffer_load_b128(rs, off, 0, 16); }
DI v2u ld8(rsrc_t rs, int off) { return __builtin_amdgcn_raw_buffer_load_b64(rs, off, 0, 16); }
DI void st16(v4u v, rsrc_t rs, int off) { __builtin_amdgcn_raw_buffer_store_b128(v, rs, off, 0, 16); }
DI void st8(v2u v, rsrc_t rs, int off) { __builtin_amdgcn_raw_buffer_store_b64(v, rs, off, 0, 16); }
DI void st4(unsigned v, rsrc_t rs, int off) { __builtin_amdgcn_raw_buffer_store_b32(v, rs, off, 0, 16); }
#define BAR() do { asm volatile("s_waitcnt lgkmcnt(0)" ::: "memory"); __builtin_amdgcn_s_barrier(); asm volatile("" ::: "memory"); } while (0)
#define VMWAIT(n) asm volatile("s_waitcnt vmcnt(%0)" :: "i"(n) : "memory")
#define FENCE() asm volatile("" ::: "memory")

DI void dma16(const void* gsrc, unsigned lds_dst) {
    unsigned keep;
    asm volatile("s_mov_b32 %0, m0\n\ts_mov_b32 m0, %2\n\ts_nop 0\n\tglobal_load_lds_dwordx4 %1, off nt\n\ts_mov_b32 m0, %0"
                 : "=&s"(keep) : "v"(gsrc), "s"(lds_dst) : "memory");
}
DI void dma16s(const void* sbase, unsigned voff, unsigned lds_dst) {
    unsigned keep;
    asm volatile("s_mov_b32 %0, m0\n\ts_mov_b32 m0, %3\n\ts_nop 2\n\tglobal_load_lds_dwordx4 %1, %2 nt\n\ts_mov_b32 m0, %0"
                 : "=&s"(keep) : "v"(voff), "s"(sbase), "s"(lds_dst) : "memory");
}
DI f32x4 mfma(v4u a, v4u b, f32x4 c) {
    return __builtin_amdgcn_mfma_f32_16x16x32_bf16(__builtin_bit_cast(bf16x8, a), __builtin_bit_cast(bf16x8, b), c, 0, 0, 0);
}
DI v2u trd(LAS unsigned char* p) { return __builtin_bit_cast(v2u, __builtin_amdgcn_ds_read_tr16_b64_v4i16((LAS s16x4*)p)); }
DI v4u trd2(LAS unsigned char* p, int second) { const v2u a = trd(p), b = trd(p + second); return v4u{a.x, a.y, b.x, b.y}; }
DI unsigned pk(float a, float b) { return __builtin_bit_cast(unsigned, __builtin_convertvector(f32x2{a, b}, bf16x2)); }
DI float blo(unsigned u) { return __builtin_bit_cast(float, u << 16); }
DI float bhi(unsigned u) { return __builtin_bit_cast(float, u & 0xffff0000u); }
DI float mlo(unsigned u) { return __builtin_bit_cast(float, (u << 16) | 0x8000u); }
DI float mhi(unsigned u) { return __builtin_bit_cast(float, (u & 0xffff0000u) | 0x8000u); }
DI unsigned fbits(float f) { return __builtin_bit_cast(unsigned, f); }
DI float bitsf(unsigned u) { return __builtin_bit_cast(float, u); }
DI unsigned tagb(unsigned v, unsigned e) { v += (v >> 1) & v & TM; return (v & ~TM) | (e & TM); }
DI unsigned tagf(float f, unsigned e) { return ((fbits(f) + 0x80u) & ~0xffu) | ((e >> 8) & 0xffu); }
template <int CTRL> DI float dpp(float v) { return bitsf(__builtin_amdgcn_update_dpp(0, fbits(v), CTRL, 0xf, 0xf, true)); }
DI float quad_sum(float v) { v += dpp<0xB1>(v); v += dpp<0x4E>(v); return v; }
DI float sum16(float v) { const auto r = __builtin_amdgcn_permlane16_swap(fbits(v), fbits(v), false, false); return bitsf(r[0]) + bitsf(r[1]); }
DI float sum32(float v) { const auto r = __builtin_amdgcn_permlane32_swap(fbits(v), fbits(v), false, false); return bitsf(r[0]) + bitsf(r[1]); }
DI float max16(float v) { const auto r = __builtin_amdgcn_permlane16_swap(fbits(v), fbits(v), false, false); return fmaxf(bitsf(r[0]), bitsf(r[1])); }
DI float max32(float v) { const auto r = __builtin_amdgcn_permlane32_swap(fbits(v), fbits(v), false, false); return fmaxf(bitsf(r[0]), bitsf(r[1])); }
DI float rs16(float a, float b) { const auto r = __builtin_amdgcn_permlane16_swap(fbits(a), fbits(b), false, false); return bitsf(r[0]) + bitsf(r[1]); }
DI float rs32(float a, float b) { const auto r = __builtin_amdgcn_permlane32_swap(fbits(a), fbits(b), false, false); return bitsf(r[0]) + bitsf(r[1]); }
DI float dot2(unsigned a, unsigned b, float c) { return __builtin_amdgcn_fdot2_f32_bf16(__builtin_bit_cast(bf16x2, a), __builtin_bit_cast(bf16x2, b), c, false); }
DI unsigned or4(v4u v) { return v.x | v.y | v.z | v.w; }

template <int N, int SLEEP> DI void gather_masked(rsrc_t rs, int off, int stride, v4u pat, unsigned mask, v4u (&v)[N]) {
    unsigned need = (1u << N) - 1u;
#pragma unroll 1
    for (int n = 0; n < SPIN; ++n) {
        FENCE();
        v4u t[N];
#pragma unroll
        for (int i = 0; i < N; ++i) t[i] = ld16(rs, off + ((need >> i) & 1u ? i * stride : OOB / 2));
#pragma unroll
        for (int i = 0; i < N; ++i) {
            t[i] ^= pat;
            const bool take = (need >> i) & 1u;
            v[i] = take ? t[i] : v[i];
            if (!__any(or4(t[i]) & mask)) need &= ~(1u << i);
        }
        if (!need) break;
        if (SLEEP) __builtin_amdgcn_s_sleep(SLEEP);
    }
}
template <int N> DI void gather_all(rsrc_t rs, int off, int stride2, int stride1, v4u pat, unsigned mask, v4u (&v)[N]) {
#pragma unroll 1
    for (int n = 0; n < SPIN; ++n) {
        FENCE();
#pragma unroll
        for (int i = 0; i < N; ++i) v[i] = ld16(rs, off + (i >> 1) * stride2 + (i & 1) * stride1);
        unsigned bad = 0;
#pragma unroll
        for (int i = 0; i < N; ++i) { v[i] ^= pat; bad |= or4(v[i]); }
        if (!__any(bad & mask)) break;
    }
}
DI unsigned slab_voff(int lane) { return (lane >> 4) * 8192 + (((lane & 15) ^ ((lane >> 4) << 2)) << 4); }
template <int NS, int GS, int SS> DI void slab_frags(LAS unsigned char* img, int lane, int wv, v4u (&a)[2 * NS]) {
    const int g = lane >> 4, q = (lane >> 2) & 3, p = lane & 3;
    LAS unsigned char* b = img + ((g >> 1) * GS + 2 * (g & 1)) * KVS + q * 256 + ((4 * (wv ^ q) + p) << 4);
#pragma unroll
    for (int s = 0; s < NS; ++s)
#pragma unroll
        for (int t = 0; t < 2; ++t) a[2 * s + t] = trd2(b + s * SS * KVS + t * 8, KVS);
}
template <int NS> DI void slab_gemv(const v4u (&a)[2 * NS], LAS unsigned char* bvec, f32x4& d0, f32x4& d1) {
    d0 = f32x4{0.f, 0.f, 0.f, 0.f}; d1 = d0;
#pragma unroll
    for (int s = 0; s < NS; ++s) {
        const v4u bf = *(LAS v4u*)(bvec + s * 64);
        d0 = mfma(a[2 * s], bf, d0);
        d1 = mfma(a[2 * s + 1], bf, d1);
    }
}
DI float rbf(float f) { return blo(pk(f, f)); }
template <int NS> DI void frags128(LAS unsigned char* img, int lane, v4u (&a)[4 * NS]) {
    const int g = lane >> 4, q = (lane >> 2) & 3, p = lane & 3;
    const int sw = (q >> 1) | ((g & 1) << 1);
    LAS unsigned char* b = img + (8 * g + q) * 128 + (p >> 1) * 16 + (p & 1) * 8;
#pragma unroll
    for (int t = 0; t < 4; ++t)
#pragma unroll
        for (int s = 0; s < NS; ++s) a[t * NS + s] = trd2(b + ((t ^ sw) << 5) + s * 4096, 512);
}
DI int chunk128(int lane, int i) { return (lane & 7) ^ ((((lane >> 4) & 1) | ((i & 1) << 1)) << 1); }

#define STAMP(i) do {} while (0)

__global__ void __launch_bounds__(512, 2) fused_decode(Params p) {
    extern __shared__ __attribute__((aligned(16))) unsigned char smem[];
    LAS unsigned char* L = (LAS unsigned char*)smem;
    const unsigned lbase = (unsigned)(size_t)L;
    const int tid = threadIdx.x, lane = tid & 63, wave = __builtin_amdgcn_readfirstlane(tid >> 6), w = blockIdx.x;
    STAMP(0);
    const rsrc_t rs = __builtin_amdgcn_make_buffer_rsrc(p.ws, 0, WS_END, 0x00020000);
    const int xg = w & 7, r = w >> 3;
    const int h = xg * 4 + (r & 3), j8 = r >> 2;
    const int b = xg >> 1, hg = r & 1, ks = (xg & 1) * 16 + (r >> 1);
    const int i15 = lane & 15, g = lane >> 4;
    LAS unsigned char* ept = L + L_EP;
    volatile LAS unsigned* flag = (volatile LAS unsigned*)(L + L_FL);
    const int wv = wave & 3;
    const unsigned char* kv = (const unsigned char*)p.ckv + ((size_t)b * 4096 + ks * 128 + wv * 32) * 1024;
    const unsigned kvl = lbase + L_KV + wv * 32 * KVS;

    if (wave >= 4) {
        const size_t so = (size_t)(j8 * 64 + wv * 16) * 8192 + h * 256;
        const unsigned vo = slab_voff(lane);
#pragma unroll 1
        for (int i = 0; i < 4; ++i) dma16s((const unsigned char*)p.wq + so + i * 32768, vo, kvl + (28 + i) * KVS);
#pragma unroll 1
        for (int i = 0; i < 4; ++i) dma16s((const unsigned char*)p.wuk + so + i * 32768, vo, lbase + L_Q + (wv * 4 + i) * KVS);
#pragma unroll 1
        for (int i = 0; i < KV_PRE; ++i) dma16s(kv + i * 1024, lane * 16, kvl + i * KVS);
        VMWAIT(4 + KV_PRE);
    } else if (wave == 3) {
        const GAS u16* xr = (const GAS u16*)p.x + (lane & 3) * 512;
        v4u xv[4];
#pragma unroll
        for (int i = 0; i < 4; ++i) xv[i] = *(const GAS v4u*)(xr + i * 128 + (lane >> 2) * 8);
        const v2u xs = *(const GAS v2u*)(xr + j8 * 64 + (lane >> 2) * 4);
        float s1 = 0.f, s2 = 0.f;
#pragma unroll
        for (int i = 0; i < 4; ++i)
#pragma unroll
            for (int k = 0; k < 4; ++k) { s1 = dot2(xv[i][k], 0x3f803f80u, s1); s2 = dot2(xv[i][k], xv[i][k], s2); }
        s1 += dpp<0x124>(s1); s2 += dpp<0x124>(s2);
        s1 += dpp<0x128>(s1); s2 += dpp<0x128>(s2);
        s1 = sum32(sum16(s1)); s2 = sum32(sum16(s2));
        const float mean = s1 * (1.f / 512.f);
        const float var = fmaxf(s2 * (1.f / 512.f) - mean * mean, 0.f);
        const float mb = rbf(mean), rstd = __builtin_amdgcn_rcpf(rbf(__builtin_amdgcn_sqrtf(rbf(rbf(var) + 1e-5f))));
        const unsigned c0 = pk(blo(xs.x) - mb, bhi(xs.x) - mb), c1 = pk(blo(xs.y) - mb, bhi(xs.y) - mb);
        *(LAS v2u*)(L + L_XN + (lane & 3) * 144 + (lane >> 2) * 8) = v2u{pk(blo(c0) * rstd, bhi(c0) * rstd), pk(blo(c1) * rstd, bhi(c1) * rstd)};
    } else if (wave == 2) {
        const unsigned e = __builtin_amdgcn_raw_buffer_load_b32(rs, WS_EP + (lane & 31) * 4, 0, 16);
        u64 pc;
        asm volatile("s_getpc_b64 %0" : "=s"(pc));
        const rsrc_t rc = __builtin_amdgcn_make_buffer_rsrc((void*)(pc & ~127ull), 0, CODE_BYTES, 0x00020000);
        const unsigned c = __builtin_amdgcn_raw_buffer_load_b32(rc, (r * 8 + (lane & 7)) * 128, 0, 0);
        unsigned t = ((e & 1u) | ((e >> 15) & 2u)) + 1u;
        *(LAS unsigned*)(ept + (lane & 31) * 4) = (t & 1u) | ((t & 2u) << 15) | ((e + 0x100u) & 0xff00u);
        if (lane < 2) flag[lane] = 0;
        asm volatile("" :: "v"(c));
    }
    BAR();
    STAMP(1);
    if (wave < 4) {
        v4u a[4];
        slab_frags<2, 32, 64>(L + L_KV + 28 * KVS, lane, wave, a);
        const v4u e = *(LAS v4u*)(ept + ((lane & 7) << 4));
        f32x4 d0, d1;
        slab_gemv<2>(a, L + L_XN + ((lane >> 1) & 3) * 144 + g * 16, d0, d1);
        const f32x4 d = (lane & 1) ? d1 : d0;
        st16(v4u{tagf(d[0], e.x), tagf(d[1], e.y), tagf(d[2], e.z), tagf(d[3], e.w)}, rs,
             (lane & 8) ? OOB : WS_Q1 + (h * 8 + j8) * 2048 + wave * 512 + g * 128 + (lane & 7) * 16);
    } else {
        VMWAIT(KV_PRE);
    }
    BAR();
    STAMP(2);
    if (wave >= 4) {
        v4u fr[32];
        LAS unsigned char* ka = L + L_KV + (32 * wv + 8 * ((i15 >> 2) & 1) + 2 * (i15 & 3) + (i15 >> 3)) * KVS + g * 256;
#pragma unroll 1
        for (int i = KV_PRE; i < 32; ++i) { dma16s(kv + i * 1024, lane * 16, kvl + i * KVS); VMWAIT(KV_THR); }
        STAMP(3);
#pragma unroll
        for (int s = 0; s < 16; ++s) fr[s] = *(LAS v4u*)(ka + s * 16);
        VMWAIT(0);
        STAMP(4);
#pragma unroll
        for (int s = 0; s < 16; ++s) fr[16 + s] = *(LAS v4u*)(ka + 16 * KVS + s * 16);
        STAMP(5);
        BAR();
        STAMP(6);
        f32x4 s0 = {0.f, 0.f, 0.f, 0.f}, s1 = s0;
        LAS unsigned char* qa = L + L_Q + i15 * KVS + g * 256;
        v4u qf[16];
#pragma unroll
        for (int s = 0; s < 16; ++s) qf[s] = *(LAS v4u*)(qa + s * 16);
        __builtin_amdgcn_sched_barrier(0);
#pragma unroll
        for (int s = 0; s < 16; ++s) { s0 = mfma(fr[s], qf[s], s0); s1 = mfma(fr[16 + s], qf[s], s1); }
        const float wm = max32(max16(fmaxf(fmaxf(fmaxf(s0[0], s0[1]), fmaxf(s0[2], s0[3])), fmaxf(fmaxf(s1[0], s1[1]), fmaxf(s1[2], s1[3])))));
        if (lane < 16) *(LAS float*)(L + L_SM + lane * 16 + wv * 4) = wm;
        BAR();
        const f32x4 m0 = *(LAS f32x4*)(L + L_SM + i15 * 16);
        const float m = bitsf(fbits(fmaxf(fmaxf(m0[0], m0[1]), fmaxf(m0[2], m0[3]))) & ~0xffu);
#pragma unroll
        for (int k = 0; k < 4; ++k) { s0[k] = __builtin_amdgcn_exp2f(s0[k] - m); s1[k] = __builtin_amdgcn_exp2f(s1[k] - m); }
        *(LAS v4u*)(L + L_P + wv * 1024 + lane * 16) = v4u{pk(s0[0], s0[1]), pk(s0[2], s0[3]), pk(s1[0], s1[1]), pk(s1[2], s1[3])};
        BAR();
        s0 += s1;
        const float ls = sum32(sum16((s0[0] + s0[1]) + (s0[2] + s0[3])));
        if (lane < 16) *(LAS float*)(L + L_SS + lane * 16 + wv * 4) = ls;
        asm volatile("s_waitcnt lgkmcnt(0)" ::: "memory");
        unsigned old = 0;
        if (lane == 0) old = __hip_atomic_fetch_add((LAS unsigned*)(L + L_FL + 4), 1u, __ATOMIC_RELAXED, __HIP_MEMORY_SCOPE_WORKGROUP);
        if (__builtin_amdgcn_readfirstlane(old) == 3) {
            FENCE();
            const f32x4 l0 = *(LAS f32x4*)(L + L_SS + i15 * 16);
            const v2u e2 = *(LAS v2u*)(ept + i15 * 8);
            st8(v2u{fbits(m) | ((e2.x >> 8) & 0xffu), tagf((l0[0] + l0[1]) + (l0[2] + l0[3]), e2.y)}, rs,
                lane < 16 ? WS_ST + ((b * 32 + ks) * 2 + hg) * 128 + lane * 8 : OOB);
        }
    } else {
        {
        const int bb = i15 >> 2, u = i15 & 3;
        v4u uk[4];
#pragma unroll
        for (int t = 0; t < 4; ++t) uk[t] = *(LAS v4u*)(L + L_Q + (4 * bb + t) * KVS + u * 256 + ((4 * (wave ^ u) + g) << 4));
        const v4u pa = (*(LAS v4u*)(ept + bb * 32) >> 8) & 0xffu, pb = (*(LAS v4u*)(ept + bb * 32 + 16) >> 8) & 0xffu;
        const v2u e2 = *(LAS v2u*)(ept + g * 32 + u * 8);
        const v4u pq = *(LAS v4u*)(ept + ((lane & 7) << 4)) & 1u;
        __builtin_amdgcn_s_sleep(Q1_DELAY);
        v4u q[4];
#pragma unroll 1
        for (int n = 0; n < SPIN; ++n) {
            FENCE();
#pragma unroll
            for (int i = 0; i < 4; ++i) q[i] = ld16(rs, WS_Q1 + (h * 8 + 2 * u + (i >> 1)) * 2048 + wave * 512 + g * 128 + bb * 32 + (i & 1) * 16);
            q[0] ^= pa; q[1] ^= pb; q[2] ^= pa; q[3] ^= pb;
            if (!__any((or4(q[0]) | or4(q[1]) | or4(q[2]) | or4(q[3])) & 0xffu)) break;
        }
        STAMP(3);
        const f32x4 qa = __builtin_bit_cast(f32x4, q[0]) + __builtin_bit_cast(f32x4, q[2]), qb = __builtin_bit_cast(f32x4, q[1]) + __builtin_bit_cast(f32x4, q[3]);
        const v4u qf = {pk(quad_sum(qa[0]), quad_sum(qa[1])), pk(quad_sum(qa[2]), quad_sum(qa[3])), pk(quad_sum(qb[0]), quad_sum(qb[1])), pk(quad_sum(qb[2]), quad_sum(qb[3]))};
        f32x4 d[4];
#pragma unroll
        for (int t = 0; t < 4; ++t) d[t] = mfma(uk[t], qf, f32x4{0.f, 0.f, 0.f, 0.f});
        *(LAS f32x4*)(L + L_P + wave * 1024 + lane * 16) = (u & 2) ? ((u & 1) ? d[3] : d[2]) : ((u & 1) ? d[1] : d[0]);
        asm volatile("s_waitcnt lgkmcnt(0)" ::: "memory");
        if (lane == 0) __hip_atomic_fetch_add((LAS unsigned*)(L + L_FL), 1u, __ATOMIC_RELAXED, __HIP_MEMORY_SCOPE_WORKGROUP);
#pragma unroll 1
        for (int n = 0; n < 64 * SPIN && flag[0] < 4; ++n) {}
        FENCE();
        if (wave == 0) {
            f32x4 o = *(LAS f32x4*)(L + L_P + lane * 16);
#pragma unroll
            for (int i = 1; i < 4; ++i) o += *(LAS f32x4*)(L + L_P + i * 1024 + lane * 16);
            o *= 0.08838834764831845f * 1.4426950408889634f;
            unsigned o0 = pk(o[0], o[1]), o1 = pk(o[2], o[3]);
            o0 += (o0 >> 1) & o0 & 1u; o1 += (o1 >> 1) & o1 & 1u;
            st8(v2u{(o0 & ~1u) | (e2.x & 1u), (o1 & ~1u) | (e2.y & 1u)}, rs, WS_QA + (bb * 32 + h) * 1024 + j8 * 128 + g * 32 + u * 8);
        }
        STAMP(4);
        __builtin_amdgcn_s_sleep(QA_DELAY);
        gather_masked<4, 4>(rs, WS_QA + (b * 32 + hg * 16 + wave * 4) * 1024 + lane * 16, 1024, pq, 1u, q);
#pragma unroll
        for (int i = 0; i < 4; ++i) *(LAS v4u*)(L + L_Q + (wave * 4 + i) * KVS + lane * 16) = q[i];
        }
        STAMP(5);
        BAR();
        STAMP(6);
        v4u fr[32];
        LAS unsigned char* va = L + L_KV + (8 * (g & 1) + 2 * (i15 >> 2) + (g >> 1)) * KVS + wave * 256 + (lane & 3) * 8;
#pragma unroll
        for (int t = 0; t < 4; ++t)
#pragma unroll
            for (int c = 0; c < 8; ++c) fr[t * 8 + c] = trd2(va + t * 32 * KVS + c * 32, 16 * KVS);
        const v4u e4 = *(LAS v4u*)(ept + ((lane & 7) << 4));
        const int ob = WS_O + ((b * 32 + hg * 16 + 4 * g + (i15 >> 3)) * 32 + ks) * 1024 + wave * 256 + (lane & 7) * 16;
        __builtin_amdgcn_s_barrier();
        BAR();
        STAMP(7);
        v4u pf[4];
#pragma unroll
        for (int t = 0; t < 4; ++t) pf[t] = *(LAS v4u*)(L + L_P + t * 1024 + lane * 16);
#pragma unroll
        for (int hf = 0; hf < 2; ++hf) {
            f32x4 o[4];
#pragma unroll
            for (int c = 0; c < 4; ++c) o[c] = f32x4{0.f, 0.f, 0.f, 0.f};
#pragma unroll
            for (int t = 0; t < 4; ++t)
#pragma unroll
                for (int c = 0; c < 4; ++c) o[c] = mfma(pf[t], fr[t * 8 + hf * 4 + c], o[c]);
#pragma unroll
            for (int k = 0; k < 4; k += 2) {
                const unsigned a0 = pk(o[0][k], o[1][k]), a1 = pk(o[2][k], o[3][k]), b0 = pk(o[0][k + 1], o[1][k + 1]), b1 = pk(o[2][k + 1], o[3][k + 1]);
                v4u u;
                u.x = __builtin_amdgcn_update_dpp(a0, b0, 0x128, 0xf, 0xc, false);
                u.y = __builtin_amdgcn_update_dpp(a1, b1, 0x128, 0xf, 0xc, false);
                u.z = __builtin_amdgcn_update_dpp(b0, a0, 0x128, 0xf, 0x3, false);
                u.w = __builtin_amdgcn_update_dpp(b1, a1, 0x128, 0xf, 0x3, false);
                st16((u & ~TM) | (e4 & TM), rs, ob + k * 32768 + hf * 128);
            }
        }
    }
    STAMP(8);
    BAR();
    if (r < 16) {
        const int cq = r >> 2;
#pragma unroll 1
        for (int i = 0; i < 4; ++i) dma16s((const unsigned char*)p.wuv + (size_t)(cq * 128 + wave * 16 + i * 4) * 8192 + h * 256, slab_voff(lane), lbase + L_KV + (wave * 4 + i) * KVS);
        const int bb = wave >> 1, ln = wave & 1;
        const v4u pat = *(LAS v4u*)(ept + ((lane & 7) << 4)) & TM;
        VMWAIT(0);
        BAR();
        STAMP(9);
        v4u a[8];
        if (wave < 4) {
            slab_frags<4, 4, 8>(L + L_KV, lane, wave, a);
            const int un = ((h & 15) >> 1) ^ (lane >> 5);
            const v4u e4 = (*(LAS v4u*)(ept + un * 16) >> 8) & 0xffu;
            v4u m4[1];
            gather_all<1>(rs, WS_ST + ((wave * 32 + (lane & 31)) * 2 + (h >> 4)) * 128 + un * 16, 0, 0, e4, 0xffu, m4);
            const v2u ml = (h & 1) ? v2u{m4[0].z, m4[0].w} : v2u{m4[0].x, m4[0].y};
            float mx = bitsf(ml.x);
            mx = fmaxf(mx, dpp<0xB1>(mx)); mx = fmaxf(mx, dpp<0x4E>(mx)); mx = fmaxf(mx, dpp<0x124>(mx)); mx = fmaxf(mx, dpp<0x128>(mx));
            mx = max16(mx);
            const float wgt = __builtin_amdgcn_exp2f(bitsf(ml.x) - mx);
            float tot = quad_sum(wgt * bitsf(ml.y));
            tot += dpp<0x124>(tot); tot += dpp<0x128>(tot);
            tot = sum16(tot);
            if (lane < 32) *(LAS float*)(L + L_WT + wave * 128 + (lane & 7) * 16 + (lane >> 3) * 4) = wgt * __builtin_amdgcn_rcpf(tot);
        }
        v4u o[4];
        gather_masked<4, 0>(rs, WS_O + ((bb * 32 + h) * 32 + (lane >> 3)) * 1024 + cq * 256 + ln * 128 + (lane & 7) * 16, 8192, pat, TM, o);
        STAMP(10);
        BAR();
        const f32x4 wt = *(LAS f32x4*)(L + L_WT + bb * 128 + (lane >> 3) * 16);
        float acc[8];
#pragma unroll
        for (int k = 0; k < 4; ++k) {
            acc[2 * k] = mlo(o[0][k]) * wt[0] + mlo(o[1][k]) * wt[1] + mlo(o[2][k]) * wt[2] + mlo(o[3][k]) * wt[3];
            acc[2 * k + 1] = mhi(o[0][k]) * wt[0] + mhi(o[1][k]) * wt[1] + mhi(o[2][k]) * wt[2] + mhi(o[3][k]) * wt[3];
        }
        const float c0 = rs32(acc[0], acc[4]), c1 = rs32(acc[1], acc[5]), c2 = rs32(acc[2], acc[6]), c3 = rs32(acc[3], acc[7]);
        float e0 = rs16(c0, c2), e1 = rs16(c1, c3);
        e0 += dpp<0x128>(e0); e1 += dpp<0x128>(e1);
        const float ov = (lane & 8) ? e1 : e0;
        const int pj = lane >> 3;
        *(LAS u16*)(L + L_OB + bb * 272 + (ln * 64 + 16 * (pj & 3) + 8 * (pj >> 2) + (lane & 7)) * 2) = (u16)pk(ov, ov);
        BAR();
        if (wave < 4) {
            const v4u e = *(LAS v4u*)(ept + (((g & 1) * 4 + (lane & 3)) << 4));
            f32x4 d0, d1;
            slab_gemv<4>(a, L + L_OB + (lane & 3) * 272 + g * 16, d0, d1);
            st16(v4u{tagb(pk(d0[0], d0[1]), e.x), tagb(pk(d0[2], d0[3]), e.y), tagb(pk(d1[0], d1[1]), e.z), tagb(pk(d1[2], d1[3]), e.w)}, rs,
                 (lane & 12) ? OOB : WS_X1 + (h * 4 + cq) * 1024 + wave * 256 + g * 64 + (lane & 3) * 16);
        }
        STAMP(11);
        if (r < 8 && wave == 4) {
            const int m0 = w * 8 + ((lane >> 2) & 1) * 4, fb = lane & 3;
            const int xo = ((m0 >> 3) & 7) * 64 + fb * 16 + (m0 & 4) * 2;
            const v2u eh = *(LAS v2u*)(ept + (xo & 127)) & TM;
            const v4u e4 = (*(LAS v4u*)(ept + ((lane & 7) << 4)) >> 8) & 0xffu;
            v2u hp, xr = {0u, 0u};
            if (lane < 8) xr = *(const GAS v2u*)((const GAS u16*)p.x + fb * 512 + m0);
#pragma unroll 1
            for (int n = 0; n < SPIN; ++n) {
                FENCE();
                hp = ld8(rs, WS_X2 + ((lane >> 3) * 8 + (m0 >> 6)) * 512 + xo) ^ eh;
                if (!__any((hp.x | hp.y) & TM)) break;
                __builtin_amdgcn_s_sleep(4);
            }
            v4u d[6];
#pragma unroll 1
            for (int n = 0; n < SPIN; ++n) {
                FENCE();
#pragma unroll
                for (int i = 0; i < 6; ++i) d[i] = ld16(rs, (i < 5 || lane < 24) ? WS_HD + (w * 48 + i * 8) * 128 + lane * 16 : OOB);
                unsigned bad = 0;
#pragma unroll
                for (int i = 0; i < 6; ++i) { if (i < 5 || lane < 24) d[i] ^= e4; bad |= or4(d[i]); }
                if (!__any(bad & 0xffu)) break;
            }
            STAMP(12);
            f32x4 s = {blo(hp.x) + blo(xr.x), bhi(hp.x) + bhi(xr.x), blo(hp.y) + blo(xr.y), bhi(hp.y) + bhi(xr.y)};
#pragma unroll
            for (int i = 0; i < 6; ++i) s += __builtin_bit_cast(f32x4, d[i]);
#pragma unroll
            for (int k = 0; k < 4; ++k) { s[k] += dpp<0x128>(s[k]); s[k] = sum32(sum16(s[k])); }
            if (lane < 8) *(GAS v2u*)((GAS u16*)p.out + fb * 512 + m0) = v2u{pk(s[0], s[1]), pk(s[2], s[3])};
            if (w == 0 && lane < 32) st4(*(LAS unsigned*)(ept + lane * 4), rs, WS_EP + lane * 4);
        }
    } else if (r < 24) {
        const int ms = r - 16;
        {
            const unsigned char* s0 = (const unsigned char*)(p.wo + (size_t)(xg * 512 + wave * 64 + (lane >> 3)) * 512 + ms * 64);
#pragma unroll 1
            for (int i = 0; i < 8; ++i) dma16(s0 + (size_t)i * 8192 + chunk128(lane, i) * 16, lbase + L_KV + wave * 8192 + i * 1024);
        }
        const int bb = i15 >> 2, u = i15 & 3;
        const int fb = (tid >> 2) & 3, fm = ((tid >> 4) & 7) * 8 + (tid & 3) * 2;
        const v4u pat = *(LAS v4u*)(ept + (((g & 1) * 4 + bb) << 4)) & TM;
        const unsigned ef = *(LAS unsigned*)(ept + (tid & 31) * 4);
        VMWAIT(0);
        v4u a[8];
        frags128<2>(L + L_KV + wave * 8192, lane, a);
        STAMP(9);
        v4u c[2];
        gather_all<2>(rs, WS_X1 + ((xg * 4 + (wave >> 1)) * 4 + u) * 1024 + (wave & 1) * 512 + g * 64 + bb * 16, 0, 256, pat, TM, c);
        STAMP(10);
#pragma unroll
        for (int t = 0; t < 4; ++t) {
            f32x4 d = mfma(a[2 * t], c[0], f32x4{0.f, 0.f, 0.f, 0.f});
            d = mfma(a[2 * t + 1], c[1], d);
            d = f32x4{quad_sum(d[0]), quad_sum(d[1]), quad_sum(d[2]), quad_sum(d[3])};
            if (u == 0) *(LAS f32x4*)(L + L_RED + wave * 1024 + bb * 256 + t * 64 + g * 16) = d;
        }
        BAR();
        if (tid < 128) {
            f32x2 s = {0.f, 0.f};
#pragma unroll
            for (int i = 0; i < 8; ++i) s += *(LAS f32x2*)(L + L_RED + i * 1024 + fb * 256 + fm * 4);
            st4(tagb(pk(s[0], s[1]), ef), rs, WS_X2 + (xg * 8 + ms) * 512 + tid * 4);
        }
        STAMP(11);
    } else if (r < 30 && xg * 6 + (r - 24) < NFF) {
        const int e = xg * 6 + (r - 24);
        {
            const size_t ro = (size_t)(wave * 64 + (lane >> 3)) * 2752 + e * 64;
#pragma unroll 1
            for (int i = 0; i < 8; ++i) {
                const int ch = chunk128(lane, i);
                dma16((const unsigned char*)((ch & 4) ? p.wu : p.wg) + ro + (size_t)i * 8 * 2752 + (ch & 3) * 16, lbase + L_KV + wave * 12288 + i * 1024);
            }
            const unsigned char* s0 = (const unsigned char*)(p.wd + (size_t)(e * 32 + (lane >> 3)) * 512 + wave * 64);
#pragma unroll 1
            for (int i = 0; i < 4; ++i) dma16(s0 + (size_t)i * 8192 + chunk128(lane, i) * 16, lbase + L_KV + wave * 12288 + 8192 + i * 1024);
        }
        const int bb = i15 >> 2, u = i15 & 3;
        const v4u pat = *(LAS v4u*)(ept + (((g & 1) * 4 + bb) << 4)) & TM;
        const v4u e4 = *(LAS v4u*)(ept + (((g & 1) * 4 + u) << 4));
        v4u xf[2];
#pragma unroll
        for (int s = 0; s < 2; ++s) xf[s] = *(const GAS v4u*)((const GAS u16*)p.x + bb * 512 + wave * 64 + s * 32 + g * 8);
        VMWAIT(0);
        v4u a[8], dn[4];
        frags128<2>(L + L_KV + wave * 12288, lane, a);
        frags128<1>(L + L_KV + wave * 12288 + 8192, lane, dn);
        {
            const v4u ones = {0x3f803f80u, 0x3f803f80u, 0x3f803f80u, 0x3f803f80u};
#pragma unroll
            for (int t = 0; t < 4; ++t) {
                f32x4 d = mfma(a[2 * t], ones, f32x4{0.f, 0.f, 0.f, 0.f});
                d = mfma(a[2 * t + 1], ones, d);
                if (i15 == 0) *(LAS f32x4*)(L + L_CS + wave * 256 + t * 64 + g * 16) = d;
            }
        }
        BAR();
        const int ff = tid & 31, fb = (tid >> 5) & 3;
        float csg = 0.f, csu = 0.f;
#pragma unroll
        for (int i = 0; i < 8; ++i) { csg += *(LAS float*)(L + L_CS + i * 256 + ff * 4); csu += *(LAS float*)(L + L_CS + i * 256 + 128 + ff * 4); }
        STAMP(9);
        v4u c[4];
        gather_all<4>(rs, WS_X2 + (u * 8 + wave) * 512 + g * 64 + bb * 16, 4 * 8 * 512, 256, pat, TM, c);
        STAMP(10);
        v4u hb[2];
        float s1 = 0.f, s2 = 0.f;
#pragma unroll
        for (int s = 0; s < 2; ++s)
#pragma unroll
            for (int k = 0; k < 4; ++k) {
                const float lo = quad_sum(blo(c[s][k]) + blo(c[2 + s][k])) + blo(xf[s][k]), hi = quad_sum(bhi(c[s][k]) + bhi(c[2 + s][k])) + bhi(xf[s][k]);
                hb[s][k] = pk(lo, hi);
                s1 = dot2(hb[s][k], 0x3f803f80u, s1); s2 = dot2(hb[s][k], hb[s][k], s2);
            }
#pragma unroll
        for (int t = 0; t < 4; ++t) {
            f32x4 d = mfma(a[2 * t], hb[0], f32x4{0.f, 0.f, 0.f, 0.f});
            d = mfma(a[2 * t + 1], hb[1], d);
            if (u == 0) *(LAS f32x4*)(L + L_RED + wave * 1024 + bb * 256 + t * 64 + g * 16) = d;
        }
        s1 = sum32(sum16(s1)); s2 = sum32(sum16(s2));
        if (lane < 16 && u == 0) *(LAS f32x2*)(L + L_STAT + bb * 64 + wave * 8) = f32x2{s1, s2};
        BAR();
        if (tid < 128) {
            float gt = 0.f, up = 0.f;
#pragma unroll
            for (int i = 0; i < 8; ++i) { gt += *(LAS float*)(L + L_RED + i * 1024 + fb * 256 + ff * 4); up += *(LAS float*)(L + L_RED + i * 1024 + fb * 256 + 128 + ff * 4); }
            f32x4 st = {0.f, 0.f, 0.f, 0.f};
#pragma unroll
            for (int i = 0; i < 4; ++i) st += *(LAS f32x4*)(L + L_STAT + fb * 64 + i * 16);
            const float mean = (st[0] + st[2]) * (1.f / 512.f);
            const float rstd = __builtin_amdgcn_rsqf(fmaxf((st[1] + st[3]) * (1.f / 512.f) - mean * mean, 0.f) + 1e-5f);
            gt = (gt - mean * csg) * rstd; up = (up - mean * csu) * rstd;
            const float act = gt * __builtin_amdgcn_rcpf(1.f + __builtin_amdgcn_exp2f(gt * -1.4426950408889634f)) * up;
            *(LAS u16*)(L + L_ACT + fb * 80 + ff * 2) = (u16)pk(act, act);
        }
        BAR();
        {
            const v4u af = *(LAS v4u*)(L + L_ACT + u * 80 + g * 16);
            f32x4 d[4];
#pragma unroll
            for (int t = 0; t < 4; ++t) d[t] = mfma(dn[t], af, f32x4{0.f, 0.f, 0.f, 0.f});
            const f32x4 o = (bb & 2) ? ((bb & 1) ? d[3] : d[2]) : ((bb & 1) ? d[1] : d[0]);
            st16(v4u{tagf(o[0], e4.x), tagf(o[1], e4.y), tagf(o[2], e4.z), tagf(o[3], e4.w)}, rs,
                 WS_HD + (((wave * 4 + bb) * 2 + (g >> 1)) * 48 + e) * 128 + (g & 1) * 64 + u * 16);
        }
        STAMP(11);
    }
}
}

extern "C" void kernel_launch(void* const* d_in, const int* in_sizes, int n_in, void* d_out, int out_size, void* d_ws, size_t ws_size,
                              hipStream_t stream) {
    Params p;
    p.x = (const u16*)d_in[0]; p.ckv = (const u16*)d_in[1]; p.wq = (const u16*)d_in[2]; p.wuk = (const u16*)d_in[3];
    p.wuv = (const u16*)d_in[4]; p.wo = (const u16*)d_in[5]; p.wg = (const u16*)d_in[7]; p.wu = (const u16*)d_in[8]; p.wd = (const u16*)d_in[9];
    p.ws = (unsigned char*)d_ws; p.out = (u16*)d_out;
    (void)hipFuncSetAttribute((const void*)fused_decode, hipFuncAttributeMaxDynamicSharedMemorySize, LDS_BYTES);
    hipLaunchKernelGGL(fused_decode, dim3(256), dim3(512), LDS_BYTES, stream, p);
}
```

```cpp
#define HOLD 16
#define Q1_DELAY 24
#include <hip/hip_runtime.h>
#include <stdint.h>

#define GAS __attribute__((address_space(1)))
#define LAS __attribute__((address_space(3)))
typedef unsigned v4u __attribute__((ext_vector_type(4)));
typedef unsigned v2u __attribute__((ext_vector_type(2)));
typedef float f32x4 __attribute__((ext_vector_type(4)));
typedef float f32x2 __attribute__((ext_vector_type(2)));
typedef short s16x4 __attribute__((ext_vector_type(4)));
typedef short s16x8 __attribute__((ext_vector_type(8)));
typedef __bf16 bf16x2 __attribute__((ext_vector_type(2)));
typedef __bf16 bf16x8 __attribute__((ext_vector_type(8)));
typedef unsigned short u16;
typedef unsigned long long u64;
typedef __amdgpu_buffer_rsrc_t rsrc_t;
#define DI __device__ __forceinline__

namespace {
constexpr int NFF = 43;
constexpr int WS_EP = 0;
constexpr int WS_Q1 = 4096;
constexpr int WS_QA = WS_Q1 + 524288;
constexpr int WS_ST = WS_QA + 131072;
constexpr int WS_X1 = WS_ST + 32768;
constexpr int WS_X2 = WS_X1 + 131072;
constexpr int WS_HD = WS_X2 + 32768;
constexpr int WS_O = WS_HD + 64 * 48 * 128;
constexpr int WS_END = WS_O + (4 << 20);
constexpr int KVS = 1040;
constexpr int L_KV = 0, L_Q = 128 * KVS, L_P = 144 * KVS;
constexpr int L_EP = L_P + 4096, L_XN = L_EP + 128, L_SM = L_XN + 4 * 144, L_SS = L_SM + 512, L_FL = L_SS + 1024, LDS_BYTES = L_FL + 64;
constexpr int L_OB = L_Q, L_WT = L_Q + 2048, L_RED = L_Q + 4096, L_CS = L_Q + 4096 + 8704, L_STAT = L_CS + 2304, L_ACT = L_STAT + 256;
static_assert(L_ACT + 4 * 80 + 64 <= L_P, "tail scratch must fit the q rows");
static_assert(LDS_BYTES <= 163840, "lds");
constexpr unsigned TM = 0x00010001u;
constexpr int SPIN = 1 << 14;
constexpr int OOB = 0x7ffffff0;
#define CODE_BYTES 16384
#define KV_PRE 8
#define QA_DELAY 36
#define ST_SLEEP 4
#define G_SLEEP 1
#define EARLY_PRE 0
#define KV_PRE0 4
#define ST_DELAY 14
#define KV_THR 8

#define SLEEP(n) __builtin_amdgcn_s_sleep(n)
#define T_Q1 Q1_DELAY
#define T_QA QA_DELAY
#define T_O O_DELAY
#define T_IND IND_MODE
#define T_HOLD HOLD
#define THROTTLE() VMWAIT(KV_THR)
#define O_DELAY 7
#define IND_MODE 0

struct Params {
    const u16 *x, *ckv, *wq, *wuk, *wuv, *wo, *wg, *wu, *wd;
    unsigned char* ws;
    u16* out;
};

DI v4u ld16(rsrc_t rs, int off) { return __builtin_amdgcn_raw_buffer_load_b128(rs, off, 0, 16); }
DI v2u ld8(rsrc_t rs, int off) { return __builtin_amdgcn_raw_buffer_load_b64(rs, off, 0, 16); }
DI void st16(v4u v, rsrc_t rs, int off) { __builtin_amdgcn_raw_buffer_store_b128(v, rs, off, 0, 16); }
DI void st8(v2u v, rsrc_t rs, int off) { __builtin_amdgcn_raw_buffer_store_b64(v, rs, off, 0, 16); }
DI void st4(unsigned v, rsrc_t rs, int off) { __builtin_amdgcn_raw_buffer_store_b32(v, rs, off, 0, 16); }
#define BAR() do { asm volatile("s_waitcnt lgkmcnt(0)" ::: "memory"); __builtin_amdgcn_s_barrier(); asm volatile("" ::: "memory"); } while (0)
#define RAWBAR() do { asm volatile("" ::: "memory"); PSTAMP(10); __builtin_amdgcn_s_barrier(); asm volatile("" ::: "memory"); PSTAMP(14); } while (0)
#define VMWAIT(n) asm volatile("s_waitcnt vmcnt(%0)" :: "i"(n) : "memory")
#define FENCE() asm volatile("" ::: "memory")

DI void dma16(const void* gsrc, unsigned lds_dst) {
    unsigned keep;
    asm volatile("s_mov_b32 %0, m0\n\ts_mov_b32 m0, %2\n\ts_nop 0\n\tglobal_load_lds_dwordx4 %1, off nt\n\ts_mov_b32 m0, %0"
                 : "=&s"(keep) : "v"(gsrc), "s"(lds_dst) : "memory");
}
DI void dma16s(const void* sbase, unsigned voff, unsigned lds_dst) {
    asm volatile("s_mov_b32 m0, %2\n\ts_nop 3\n\tglobal_load_lds_dwordx4 %0, %1 nt" :: "v"(voff), "s"(sbase), "s"(lds_dst) : "memory");
}
DI f32x4 mfma(v4u a, v4u b, f32x4 c) {
    return __builtin_amdgcn_mfma_f32_16x16x32_bf16(__builtin_bit_cast(bf16x8, a), __builtin_bit_cast(bf16x8, b), c, 0, 0, 0);
}
DI v2u trd(LAS unsigned char* p) { return __builtin_bit_cast(v2u, __builtin_amdgcn_ds_read_tr16_b64_v4i16((LAS s16x4*)p)); }
DI v4u trd2(LAS unsigned char* p, int second) { const v2u a = trd(p), b = trd(p + second); return v4u{a.x, a.y, b.x, b.y}; }
DI unsigned pk(float a, float b) { return __builtin_bit_cast(unsigned, __builtin_convertvector(f32x2{a, b}, bf16x2)); }
DI float blo(unsigned u) { return __builtin_bit_cast(float, u << 16); }
DI float bhi(unsigned u) { return __builtin_bit_cast(float, u & 0xffff0000u); }
DI float mlo(unsigned u) { return __builtin_bit_cast(float, (u << 16) | 0x8000u); }
DI float mhi(unsigned u) { return __builtin_bit_cast(float, u); }
DI unsigned fbits(float f) { return __builtin_bit_cast(unsigned, f); }
DI float bitsf(unsigned u) { return __builtin_bit_cast(float, u); }
DI unsigned tagb(unsigned v, unsigned e) { v += (v >> 1) & v & TM; return (v & ~TM) | (e & TM); }
DI unsigned tagf(float f, unsigned e) { return ((fbits(f) + 0x80u) & ~0xffu) | ((e >> 8) & 0xffu); }
template <int CTRL> DI float dpp(float v) { return bitsf(__builtin_amdgcn_update_dpp(0, fbits(v), CTRL, 0xf, 0xf, true)); }
DI float quad_sum(float v) { v += dpp<0xB1>(v); v += dpp<0x4E>(v); return v; }
DI float col_sum(float v) { v += dpp<0x124>(v); v += dpp<0x128>(v); return v; }
DI float sum16(float v) { const auto r = __builtin_amdgcn_permlane16_swap(fbits(v), fbits(v), false, false); return bitsf(r[0]) + bitsf(r[1]); }
DI float sum32(float v) { const auto r = __builtin_amdgcn_permlane32_swap(fbits(v), fbits(v), false, false); return bitsf(r[0]) + bitsf(r[1]); }
DI float max16(float v) { const auto r = __builtin_amdgcn_permlane16_swap(fbits(v), fbits(v), false, false); return fmaxf(bitsf(r[0]), bitsf(r[1])); }
DI float max32(float v) { const auto r = __builtin_amdgcn_permlane32_swap(fbits(v), fbits(v), false, false); return fmaxf(bitsf(r[0]), bitsf(r[1])); }
DI float rs16(float a, float b) { const auto r = __builtin_amdgcn_permlane16_swap(fbits(a), fbits(b), false, false); return bitsf(r[0]) + bitsf(r[1]); }
DI float rs32(float a, float b) { const auto r = __builtin_amdgcn_permlane32_swap(fbits(a), fbits(b), false, false); return bitsf(r[0]) + bitsf(r[1]); }
DI float dot2(unsigned a, unsigned b, float c) { return __builtin_amdgcn_fdot2_f32_bf16(__builtin_bit_cast(bf16x2, a), __builtin_bit_cast(bf16x2, b), c, false); }
#define PIN(x) asm volatile("" : "+v"(x))
DI f32x4 rs8x4(f32x4 a, f32x4 b) {
    f32x4 r;
    asm volatile("s_nop 7\n\t"
                 "v_add_f32_dpp %0, %4, %4 row_ror:8 row_mask:0xf bank_mask:0x3\n\tv_add_f32_dpp %1, %5, %5 row_ror:8 row_mask:0xf bank_mask:0x3\n\t"
                 "v_add_f32_dpp %2, %6, %6 row_ror:8 row_mask:0xf bank_mask:0x3\n\tv_add_f32_dpp %3, %7, %7 row_ror:8 row_mask:0xf bank_mask:0x3\n\t"
                 "v_add_f32_dpp %0, %8, %8 row_ror:8 row_mask:0xf bank_mask:0xc\n\tv_add_f32_dpp %1, %9, %9 row_ror:8 row_mask:0xf bank_mask:0xc\n\t"
                 "v_add_f32_dpp %2, %10, %10 row_ror:8 row_mask:0xf bank_mask:0xc\n\tv_add_f32_dpp %3, %11, %11 row_ror:8 row_mask:0xf bank_mask:0xc"
                 : "=&v"(r[0]), "=&v"(r[1]), "=&v"(r[2]), "=&v"(r[3])
                 : "v"(a[0]), "v"(a[1]), "v"(a[2]), "v"(a[3]), "v"(b[0]), "v"(b[1]), "v"(b[2]), "v"(b[3]));
    return r;
}
DI f32x4 rs4x4(f32x4 a, f32x4 b) {
    f32x4 r;
    asm volatile("s_nop 1\n\t"
                 "v_add_f32_dpp %0, %4, %4 row_shl:4 row_mask:0xf bank_mask:0x5\n\tv_add_f32_dpp %1, %5, %5 row_shl:4 row_mask:0xf bank_mask:0x5\n\t"
                 "v_add_f32_dpp %2, %6, %6 row_shl:4 row_mask:0xf bank_mask:0x5\n\tv_add_f32_dpp %3, %7, %7 row_shl:4 row_mask:0xf bank_mask:0x5\n\t"
                 "v_add_f32_dpp %0, %8, %8 row_shr:4 row_mask:0xf bank_mask:0xa\n\tv_add_f32_dpp %1, %9, %9 row_shr:4 row_mask:0xf bank_mask:0xa\n\t"
                 "v_add_f32_dpp %2, %10, %10 row_shr:4 row_mask:0xf bank_mask:0xa\n\tv_add_f32_dpp %3, %11, %11 row_shr:4 row_mask:0xf bank_mask:0xa"
                 : "=&v"(r[0]), "=&v"(r[1]), "=&v"(r[2]), "=&v"(r[3])
                 : "v"(a[0]), "v"(a[1]), "v"(a[2]), "v"(a[3]), "v"(b[0]), "v"(b[1]), "v"(b[2]), "v"(b[3]));
    return r;
}
DI void add4x8(f32x4& a, f32x4& b) {
    asm volatile("s_nop 1\n\t"
                 "v_add_f32_dpp %0, %0, %0 row_shl:4 row_mask:0xf bank_mask:0x5\n\tv_add_f32_dpp %1, %1, %1 row_shl:4 row_mask:0xf bank_mask:0x5\n\t"
                 "v_add_f32_dpp %2, %2, %2 row_shl:4 row_mask:0xf bank_mask:0x5\n\tv_add_f32_dpp %3, %3, %3 row_shl:4 row_mask:0xf bank_mask:0x5\n\t"
                 "v_add_f32_dpp %4, %4, %4 row_shl:4 row_mask:0xf bank_mask:0x5\n\tv_add_f32_dpp %5, %5, %5 row_shl:4 row_mask:0xf bank_mask:0x5\n\t"
                 "v_add_f32_dpp %6, %6, %6 row_shl:4 row_mask:0xf bank_mask:0x5\n\tv_add_f32_dpp %7, %7, %7 row_shl:4 row_mask:0xf bank_mask:0x5"
                 : "+v"(a[0]), "+v"(a[1]), "+v"(a[2]), "+v"(a[3]), "+v"(b[0]), "+v"(b[1]), "+v"(b[2]), "+v"(b[3]));
}
DI void colsum8(f32x4& a, f32x4& b) {
    asm volatile("s_nop 1\n\t"
                 "v_add_f32_dpp %0, %0, %0 row_ror:4 row_mask:0xf bank_mask:0xf\n\tv_add_f32_dpp %1, %1, %1 row_ror:4 row_mask:0xf bank_mask:0xf\n\t"
                 "v_add_f32_dpp %2, %2, %2 row_ror:4 row_mask:0xf bank_mask:0xf\n\tv_add_f32_dpp %3, %3, %3 row_ror:4 row_mask:0xf bank_mask:0xf\n\t"
                 "v_add_f32_dpp %4, %4, %4 row_ror:4 row_mask:0xf bank_mask:0xf\n\tv_add_f32_dpp %5, %5, %5 row_ror:4 row_mask:0xf bank_mask:0xf\n\t"
                 "v_add_f32_dpp %6, %6, %6 row_ror:4 row_mask:0xf bank_mask:0xf\n\tv_add_f32_dpp %7, %7, %7 row_ror:4 row_mask:0xf bank_mask:0xf\n\t"
                 "v_add_f32_dpp %0, %0, %0 row_ror:8 row_mask:0xf bank_mask:0xf\n\tv_add_f32_dpp %1, %1, %1 row_ror:8 row_mask:0xf bank_mask:0xf\n\t"
                 "v_add_f32_dpp %2, %2, %2 row_ror:8 row_mask:0xf bank_mask:0xf\n\tv_add_f32_dpp %3, %3, %3 row_ror:8 row_mask:0xf bank_mask:0xf\n\t"
                 "v_add_f32_dpp %4, %4, %4 row_ror:8 row_mask:0xf bank_mask:0xf\n\tv_add_f32_dpp %5, %5, %5 row_ror:8 row_mask:0xf bank_mask:0xf\n\t"
                 "v_add_f32_dpp %6, %6, %6 row_ror:8 row_mask:0xf bank_mask:0xf\n\tv_add_f32_dpp %7, %7, %7 row_ror:8 row_mask:0xf bank_mask:0xf"
                 : "+v"(a[0]), "+v"(a[1]), "+v"(a[2]), "+v"(a[3]), "+v"(b[0]), "+v"(b[1]), "+v"(b[2]), "+v"(b[3]));
}
DI f32x4 unp01(v4u v) { return f32x4{blo(v.x), bhi(v.x), blo(v.y), bhi(v.y)}; }
DI f32x4 unp23(v4u v) { return f32x4{blo(v.z), bhi(v.z), blo(v.w), bhi(v.w)}; }
DI unsigned or4(v4u v) { return v.x | v.y | v.z | v.w; }

template <int N, int SLEEP> DI void gather_masked(rsrc_t rs, int off, int stride, v4u pat, unsigned mask, v4u (&v)[N]) {
    unsigned need = (1u << N) - 1u;
#pragma unroll 1
    for (int n = 0; n < SPIN; ++n) {
        FENCE();
        v4u t[N];
#pragma unroll
        for (int i = 0; i < N; ++i) t[i] = ld16(rs, off + ((need >> i) & 1u ? i * stride : OOB / 2));
#pragma unroll
        for (int i = 0; i < N; ++i) {
            t[i] ^= pat;
            const bool take = (need >> i) & 1u;
            v[i] = take ? t[i] : v[i];
            if (!__any(or4(t[i]) & mask)) need &= ~(1u << i);
        }
        if (!need) break;
        if (SLEEP) __builtin_amdgcn_s_sleep(SLEEP);
    }
}
template <int N> DI void gather_all(rsrc_t rs, int off, int stride2, int stride1, v4u pat, unsigned mask, v4u (&v)[N]) {
#pragma unroll 1
    for (int n = 0; n < SPIN; ++n) {
        FENCE();
#pragma unroll
        for (int i = 0; i < N; ++i) v[i] = ld16(rs, off + (i >> 1) * stride2 + (i & 1) * stride1);
        unsigned bad = 0;
#pragma unroll
        for (int i = 0; i < N; ++i) { v[i] ^= pat; bad |= or4(v[i]); }
        if (!__any(bad & mask)) break;
        __builtin_amdgcn_s_sleep(G_SLEEP);
    }
}
DI unsigned slab_voff(int lane) { return (lane >> 4) * 8192 + (((lane & 15) ^ ((lane >> 4) << 2)) << 4); }
template <int NS, int GS, int SS> DI void slab_frags(LAS unsigned char* img, int lane, int wv, v4u (&a)[2 * NS]) {
    const int g = lane >> 4, q = (lane >> 2) & 3, p = lane & 3;
    LAS unsigned char* b = img + ((g >> 1) * GS + 2 * (g & 1)) * KVS + q * 256 + ((4 * (wv ^ q) + p) << 4);
#pragma unroll
    for (int s = 0; s < NS; ++s)
#pragma unroll
        for (int t = 0; t < 2; ++t) a[2 * s + t] = trd2(b + s * SS * KVS + t * 8, KVS);
}
template <int NS> DI void slab_gemv(const v4u (&a)[2 * NS], LAS unsigned char* bvec, f32x4& d0, f32x4& d1) {
    d0 = f32x4{0.f, 0.f, 0.f, 0.f}; d1 = d0;
#pragma unroll
    for (int s = 0; s < NS; ++s) {
        const v4u bf = *(LAS v4u*)(bvec + s * 64);
        d0 = mfma(a[2 * s], bf, d0);
        d1 = mfma(a[2 * s + 1], bf, d1);
    }
}
DI float rbf(float f) { return blo(pk(f, f)); }
constexpr int PP = 1088;
DI int blk_chunk(int lane) { return (lane & 7) ^ ((lane >> 3) & 2); }
template <int NS> DI void blk_frags(LAS unsigned char* img, int lane, v4u (&a)[4 * NS]) {
    const int g = lane >> 4, q = (lane >> 2) & 3, p = lane & 3;
    LAS unsigned char* b = img + g * PP + q * 128 + p * 8;
#pragma unroll
    for (int t = 0; t < 4; ++t)
#pragma unroll
        for (int s = 0; s < NS; ++s) a[t * NS + s] = trd2(b + ((t ^ (q >> 1)) << 5) + s * 4 * PP, 512);
}

#define STAMP(i) do {} while (0)

#define PSTAMP(i) do {} while (0)
__global__ void __launch_bounds__(512, 2) fused_decode(Params p) {
    extern __shared__ __attribute__((aligned(16))) unsigned char smem[];
    LAS unsigned char* L = (LAS unsigned char*)smem;
    const unsigned lbase = (unsigned)(size_t)L;
    const int tid = threadIdx.x, lane = tid & 63, wave = __builtin_amdgcn_readfirstlane(tid >> 6), w = blockIdx.x;
    STAMP(0);
    if (tid >= 64 && tid < 72) *(LAS unsigned*)(L + L_FL + (tid - 64) * 4) = 0u;
    unsigned cpf = 0;
    if (wave == 0) {
        u64 pc;
        asm volatile("s_getpc_b64 %0" : "=s"(pc));
        const rsrc_t rc = __builtin_amdgcn_make_buffer_rsrc((void*)(pc & ~127ull), 0, CODE_BYTES, 0x00020000);
        cpf = __builtin_amdgcn_raw_buffer_load_b32(rc, (((w >> 3) & 7) * 16 + (lane & 15)) * 128, 0, 0);
    }
    const rsrc_t rs = __builtin_amdgcn_make_buffer_rsrc(p.ws, 0, WS_END, 0x00020000);
    const int xg = w & 7, r = w >> 3;
    const int h = xg * 4 + (r & 3), j8 = r >> 2;
    const int b = xg >> 1, hg = r & 1, ks = (xg & 1) * 16 + (r >> 1);
    const int i15 = lane & 15, g = lane >> 4;
    LAS unsigned char* ept = L + L_EP;
    volatile LAS unsigned* flag = (volatile LAS unsigned*)(L + L_FL);
    const int wv = wave & 3;
    const unsigned char* kv = (const unsigned char*)p.ckv + ((size_t)b * 4096 + ks * 128 + wv * 32) * 1024;
    const unsigned kvl = lbase + L_KV + wv * 32 * KVS;

    volatile LAS unsigned char* fb8 = (volatile LAS unsigned char*)flag;
    PSTAMP(9);
    v4u xv[4];
    v2u xs;
    unsigned ep = 0;
    if (wave == 3) {
        const u16* xr = p.x + g * 512 + i15 * 8;
        const u16* xq = p.x + g * 512 + j8 * 64 + i15 * 4;
        asm volatile("global_load_dwordx4 %0, %5, off\n\tglobal_load_dwordx4 %1, %5, off offset:256\n\tglobal_load_dwordx4 %2, %5, off offset:512\n\t"
                     "global_load_dwordx4 %3, %5, off offset:768\n\tglobal_load_dwordx2 %4, %6, off"
                     : "=&v"(xv[0]), "=&v"(xv[1]), "=&v"(xv[2]), "=&v"(xv[3]), "=&v"(xs) : "v"(xr), "v"(xq) : "memory");
    }
    if (wave == 2) ep = __builtin_amdgcn_raw_buffer_load_b32(rs, WS_EP + (lane & 31) * 4, 0, 16);
    const size_t so = (size_t)(j8 * 64) * 8192 + h * 256;
    const unsigned vo = slab_voff(lane);
    {
        const unsigned char* src = (const unsigned char*)p.wq + so + wave * 65536;
        const unsigned dst = lbase + L_KV + (32 * (wave >> 1) + 28 + 2 * (wave & 1)) * KVS;
        dma16s(src, vo, dst);
        dma16s(src + 32768, vo, dst + KVS);
    }
    PSTAMP(10);
    BAR();
    PSTAMP(14);
    if (__builtin_expect(wave >= 4, 1)) {
        SLEEP(T_HOLD);
#pragma unroll 1
        for (int i = 0; i < KV_PRE0; ++i) dma16s(kv + i * 1024, lane * 16, kvl + i * KVS);
        VMWAIT(KV_PRE0);
        fb8[16 + wave] = 1;
        PSTAMP(15);
#pragma unroll 1
        for (int i = 0; i < 4; ++i) dma16s((const unsigned char*)p.wuk + so + (wv * 4 + i) * 32768, vo, lbase + L_Q + (wv * 4 + i) * KVS);
#pragma unroll 1
        for (int i = KV_PRE0; i < 32; ++i) {
            if (i == 28) {
#pragma unroll 1
                for (int n = 0; n < 64 * SPIN && flag[3] != 0x01010101u; ++n) {}
            }
            dma16s(kv + i * 1024, lane * 16, kvl + i * KVS);
            THROTTLE();
            if (i == KV_PRE0 + KV_THR - 1) { fb8[24 + wv] = 1; STAMP(2); }
        }
    }
    int wave2 = wave;
    asm volatile("" : "+s"(wave2));
    const int ef_ = xg * 6 + (r - 24);
    const bool roleE = r >= 24 && r < 30 && ef_ < NFF;
    auto role_dma = [&]() {
        const GAS unsigned char* gs = r < 24 ? (const GAS unsigned char*)p.wo + (size_t)(xg * 512 + wave * 64 + (lane >> 3)) * 1024 + (r - 16) * 128 + blk_chunk(lane) * 16
            : (const GAS unsigned char*)((lane & 4) ? p.wu : p.wg) + (size_t)(wave * 64 + (lane >> 3)) * 2752 + ef_ * 64 + (blk_chunk(lane) & 3) * 16;
        const int st = r < 24 ? 8192 : 8 * 2752;
        if (r < 24 || roleE) {
#pragma unroll 1
            for (int i = 0; i < 8; ++i) dma16((const void*)(gs + (size_t)(i * st)), lbase + L_KV + (wave * 12 + i) * PP);
        }
    };
    if (wave2 >= 4) {
        v4u fr[32], pre[8];
        LAS unsigned char* ka = L + L_KV + (32 * wv + 8 * ((i15 >> 2) & 1) + 2 * (i15 & 3) + (i15 >> 3)) * KVS + g * 256;
        STAMP(3);
        const GAS unsigned char* psrc = (const GAS unsigned char*)p.x;
        int pstr = 0, pds = KVS;
        LAS unsigned char* pdst = L + L_KV + wv * 8 * KVS + lane * 16;
        if (r < 16) { psrc = (const GAS unsigned char*)p.wuv + (size_t)((r >> 2) * 128 + wv * 32) * 8192 + h * 256 + slab_voff(lane); pstr = 32768; }
        else if (roleE) {
            psrc = (const GAS unsigned char*)p.wd + (size_t)(ef_ * 32 + wv * 8 + (lane >> 3)) * 1024 + blk_chunk(lane) * 16; pstr = 128;
            pdst = L + L_KV + (8 + wv) * PP + lane * 16; pds = 12 * PP;
        }
        PIN(psrc); PIN(pdst);
#pragma unroll
        for (int s = 0; s < 16; ++s) fr[s] = *(LAS v4u*)(ka + s * 16);
        VMWAIT(EARLY_PRE ? 8 : 0);
        STAMP(4);
#pragma unroll
        for (int s = 0; s < 16; ++s) fr[16 + s] = *(LAS v4u*)(ka + 16 * KVS + s * 16);
        STAMP(5);
        BAR();
        STAMP(6);
        f32x4 s0 = {0.f, 0.f, 0.f, 0.f}, s1 = s0;
        LAS unsigned char* qa = L + L_Q + i15 * KVS + g * 256;
        __builtin_amdgcn_s_setprio(2);
        v4u qf[16];
#pragma unroll
        for (int s = 0; s < 6; ++s) qf[s] = *(LAS v4u*)(qa + s * 16);
        __builtin_amdgcn_sched_barrier(0);
#pragma unroll
        for (int s = 0; s < 16; ++s) {
            s0 = mfma(fr[s], qf[s], s0);
            if (s + 6 < 16) qf[s + 6] = *(LAS v4u*)(qa + (s + 6) * 16);
            __builtin_amdgcn_sched_barrier(0);
            s1 = mfma(fr[16 + s], qf[s], s1);
            if (!EARLY_PRE && s < 8) pre[s] = __builtin_nontemporal_load((const GAS v4u*)(psrc + (size_t)(s * pstr)));
            __builtin_amdgcn_sched_barrier(0);
        }
        const float wm = max32(max16(fmaxf(fmaxf(fmaxf(s0[0], s0[1]), fmaxf(s0[2], s0[3])), fmaxf(fmaxf(s1[0], s1[1]), fmaxf(s1[2], s1[3])))));
        if (lane < 16) *(LAS float*)(L + L_SM + lane * 16 + wv * 4) = wm;
        BAR();
        const f32x4 m0 = *(LAS f32x4*)(L + L_SM + i15 * 16);
        const float m = bitsf(fbits(fmaxf(fmaxf(m0[0], m0[1]), fmaxf(m0[2], m0[3]))) & ~0xffu);
#pragma unroll
        for (int k = 0; k < 4; ++k) { s0[k] = __builtin_amdgcn_exp2f(s0[k] - m); s1[k] = __builtin_amdgcn_exp2f(s1[k] - m); }
        *(LAS v4u*)(L + L_P + wv * 1024 + lane * 16) = v4u{pk(s0[0], s0[1]), pk(s0[2], s0[3]), pk(s1[0], s1[1]), pk(s1[2], s1[3])};
        s0 += s1;
        *(LAS float*)(L + L_SS + lane * 16 + wv * 4) = (s0[0] + s0[1]) + (s0[2] + s0[3]);
        BAR();
        __builtin_amdgcn_s_setprio(0);
        if (r >= 16) {
            BAR();
            role_dma();
        }
        if (r < 16 || roleE) {
#pragma unroll
            for (int i = 0; i < 8; ++i) *(LAS v4u*)(pdst + i * pds) = pre[i];
        }
        FENCE();
        ((volatile LAS unsigned char*)flag)[8 + wv] = 1;
    } else {
        if (wave == 3) {
            asm volatile("s_waitcnt vmcnt(2)" : "+v"(xv[0]), "+v"(xv[1]), "+v"(xv[2]), "+v"(xv[3]), "+v"(xs) :: "memory");
            float s1 = 0.f, s2 = 0.f;
#pragma unroll
            for (int i = 0; i < 4; ++i)
#pragma unroll
                for (int k = 0; k < 4; ++k) { s1 = dot2(xv[i][k], 0x3f803f80u, s1); s2 = dot2(xv[i][k], xv[i][k], s2); }
            s1 = col_sum(quad_sum(s1)); s2 = col_sum(quad_sum(s2));
            const float mean = s1 * (1.f / 512.f);
            const float var = fmaxf(s2 * (1.f / 512.f) - mean * mean, 0.f);
            const float mb = rbf(mean), rstd = __builtin_amdgcn_rcpf(rbf(__builtin_amdgcn_sqrtf(rbf(rbf(var) + 1e-5f))));
            const unsigned c0 = pk(blo(xs.x) - mb, bhi(xs.x) - mb), c1 = pk(blo(xs.y) - mb, bhi(xs.y) - mb);
            *(LAS v2u*)(L + L_XN + g * 144 + i15 * 8) = v2u{pk(blo(c0) * rstd, bhi(c0) * rstd), pk(blo(c1) * rstd, bhi(c1) * rstd)};
        } else if (wave == 2) {
            const unsigned t = ((ep & 1u) | ((ep >> 15) & 2u)) + 1u;
            *(LAS unsigned*)(ept + (lane & 31) * 4) = (t & 1u) | ((t & 2u) << 15) | ((ep + 0x100u) & 0xff00u);
        }
        asm volatile("" :: "v"(cpf));
        int q1dst = (lane & 8) ? OOB : WS_Q1 + (h * 8 + j8) * 2048 + wave * 512 + g * 128 + (lane & 7) * 16;
        PIN(q1dst);
        VMWAIT(0);
        fb8[16 + wave] = 1;
        PSTAMP(15);
#pragma unroll 1
        for (int n = 0; n < 64 * SPIN && *(volatile LAS u64*)(fb8 + 16) != 0x0101010101010101ull; ++n) {}
        FENCE();
        STAMP(1);
        {
            v4u a[4];
            slab_frags<2, 32, 64>(L + L_KV + 28 * KVS, lane, wave, a);
            const v4u e = *(LAS v4u*)(ept + ((lane & 7) << 4));
            f32x4 d0, d1;
            slab_gemv<2>(a, L + L_XN + (lane & 3) * 144 + g * 16, d0, d1);
            const f32x4 d = (lane & 4) ? d1 : d0;
            st16(v4u{tagf(d[0], e.x), tagf(d[1], e.y), tagf(d[2], e.z), tagf(d[3], e.w)}, rs, q1dst);
            FENCE();
            fb8[12 + wave] = 1;
        }
        STAMP(2);
        {
        const int u = i15 >> 2, bb = i15 & 3;
        v4u uk[4];
        LAS unsigned char* ukr = L + L_Q + 4 * u * KVS + bb * 256 + ((4 * (wave ^ bb) + g) << 4);
        const v4u pa = (*(LAS v4u*)(ept + bb * 16) >> 8) & 0xffu, pb = (*(LAS v4u*)(ept + bb * 16 + 64) >> 8) & 0xffu;
        const v2u e2 = *(LAS v2u*)(ept + i15 * 8);
        const v4u pq = *(LAS v4u*)(ept + ((lane & 7) << 4)) & 1u;
        LAS unsigned char* rw = L + L_P + wave * 1024 + bb * 256 + g * 64 + u * 16;
        int qdst = WS_QA + (g * 32 + h) * 1024 + j8 * 128 + i15 * 8;
        PIN(rw); PIN(qdst);
        SLEEP(T_Q1);
        v4u q[4];
#pragma unroll 1
        for (int n = 0; n < SPIN; ++n) {
            FENCE();
#pragma unroll
            for (int i = 0; i < 4; ++i) q[i] = ld16(rs, WS_Q1 + (h * 8 + 2 * u + (i >> 1)) * 2048 + wave * 512 + g * 128 + (i & 1) * 64 + bb * 16);
#pragma unroll 1
            for (int k = 0; k < 64 * SPIN && flag[6] != 0x01010101u; ++k) {}
            FENCE();
#pragma unroll
            for (int t = 0; t < 4; ++t) uk[t] = *(LAS v4u*)(ukr + t * KVS);
            q[0] ^= pa; q[1] ^= pb; q[2] ^= pa; q[3] ^= pb;
            if (!__any((or4(q[0]) | or4(q[1]) | or4(q[2]) | or4(q[3])) & 0xffu)) break;
        }
        STAMP(3);
        f32x4 qa = __builtin_bit_cast(f32x4, q[0]) + __builtin_bit_cast(f32x4, q[2]), qb = __builtin_bit_cast(f32x4, q[1]) + __builtin_bit_cast(f32x4, q[3]);
        colsum8(qa, qb);
        const v4u qf = {pk(qa[0], qa[1]), pk(qa[2], qa[3]), pk(qb[0], qb[1]), pk(qb[2], qb[3])};
        f32x4 d[4];
#pragma unroll
        for (int t = 0; t < 4; ++t) d[t] = mfma(uk[t], qf, f32x4{0.f, 0.f, 0.f, 0.f});
        *(LAS f32x4*)rw = (u & 2) ? ((u & 1) ? d[3] : d[2]) : ((u & 1) ? d[1] : d[0]);
        FENCE();
        ((volatile LAS unsigned char*)flag)[wave] = 1;
#pragma unroll 1
        for (int n = 0; n < 64 * SPIN && flag[0] != 0x01010101u; ++n) {}
        FENCE();
        if (wave == 0) {
            f32x4 o = *(LAS f32x4*)(L + L_P + lane * 16);
#pragma unroll
            for (int i = 1; i < 4; ++i) o += *(LAS f32x4*)(L + L_P + i * 1024 + lane * 16);
            o *= 0.08838834764831845f * 1.4426950408889634f;
            unsigned o0 = pk(o[0], o[1]), o1 = pk(o[2], o[3]);
            o0 += (o0 >> 1) & o0 & 1u; o1 += (o1 >> 1) & o1 & 1u;
            st8(v2u{(o0 & ~1u) | (e2.x & 1u), (o1 & ~1u) | (e2.y & 1u)}, rs, qdst);
        }
        STAMP(4);
        SLEEP(T_QA);
        if (T_IND) {
            if (wave == 1) {
#pragma unroll 1
                for (int n = 0; n < SPIN; ++n) {
                    FENCE();
                    const v4u t = ld16(rs, WS_QA + (b * 32 + hg * 16 + (lane >> 3) * 2 + 1) * 1024 + 7 * 128 + (lane & 7) * 16) ^ pq;
                    if (!__any(or4(t) & 1u)) break;
                    __builtin_amdgcn_s_sleep(4);
                }
                flag[1] = 1;
            } else {
#pragma unroll 1
                for (int n = 0; n < 64 * SPIN && flag[1] == 0; ++n) __builtin_amdgcn_s_sleep(1);
            }
            SLEEP(T_IND > 0 ? T_IND - 1 : 0);
        }
        gather_masked<4, 4>(rs, WS_QA + (b * 32 + hg * 16 + wave * 4) * 1024 + lane * 16, 1024, pq, 1u, q);
#pragma unroll
        for (int i = 0; i < 4; ++i) *(LAS v4u*)(L + L_Q + (wave * 4 + i) * KVS + lane * 16) = q[i];
        }
        STAMP(5);
        BAR();
        STAMP(6);
        v4u fr[32];
        LAS unsigned char* va = L + L_KV + (8 * (g & 1) + 2 * (i15 >> 2) + (g >> 1)) * KVS + wave * 256 + (lane & 3) * 8;
#pragma unroll
        for (int t = 0; t < 4; ++t)
#pragma unroll
            for (int c = 0; c < 8; ++c) fr[t * 8 + c] = trd2(va + t * 32 * KVS + c * 32, 16 * KVS);
        const v4u e4 = *(LAS v4u*)(ept + ((lane & 7) << 4));
        const int ob = WS_O + ((b * 32 + hg * 16 + 4 * g + (i15 >> 3)) * 32 + ks) * 1024 + wave * 256 + (lane & 7) * 16;
        const v2u e2 = *(LAS v2u*)(ept + i15 * 8);
        int sdst = tid < 16 ? WS_ST + ((b * 32 + ks) * 2 + hg) * 128 + lane * 8 : OOB;
        PIN(sdst);
        __builtin_amdgcn_s_barrier();
        FENCE();
        const f32x4 m0 = *(LAS f32x4*)(L + L_SM + i15 * 16);
        unsigned mtag = (fbits(fmaxf(fmaxf(m0[0], m0[1]), fmaxf(m0[2], m0[3]))) & ~0xffu) | ((e2.x >> 8) & 0xffu);
        PIN(mtag);
        BAR();
        STAMP(7);
        v4u pf[4];
#pragma unroll
        for (int t = 0; t < 4; ++t) pf[t] = *(LAS v4u*)(L + L_P + t * 1024 + lane * 16);
        {
            const f32x4 l0 = *(LAS f32x4*)(L + L_SS + lane * 16);
            const float l = sum32(sum16((l0[0] + l0[1]) + (l0[2] + l0[3])));
            st8(v2u{mtag, tagf(l, e2.y)}, rs, sdst);
        }
#pragma unroll
        for (int hf = 0; hf < 2; ++hf) {
            f32x4 o[4];
#pragma unroll
            for (int c = 0; c < 4; ++c) o[c] = f32x4{0.f, 0.f, 0.f, 0.f};
#pragma unroll
            for (int t = 0; t < 4; ++t)
#pragma unroll
                for (int c = 0; c < 4; ++c) o[c] = mfma(pf[t], fr[t * 8 + hf * 4 + c], o[c]);
#pragma unroll
            for (int k = 0; k < 4; k += 2) {
                const unsigned a0 = pk(o[0][k], o[1][k]), a1 = pk(o[2][k], o[3][k]), b0 = pk(o[0][k + 1], o[1][k + 1]), b1 = pk(o[2][k + 1], o[3][k + 1]);
                v4u u;
                u.x = __builtin_amdgcn_update_dpp(a0, b0, 0x128, 0xf, 0xc, false);
                u.y = __builtin_amdgcn_update_dpp(a1, b1, 0x128, 0xf, 0xc, false);
                u.z = __builtin_amdgcn_update_dpp(b0, a0, 0x128, 0xf, 0x3, false);
                u.w = __builtin_amdgcn_update_dpp(b1, a1, 0x128, 0xf, 0x3, false);
                                st16((u & ~TM) | (e4 & TM), rs, ob + k * 32768 + hf * 128);
            }
        }
        STAMP(8);
        if (r >= 16) {
            BAR();
            role_dma();
        }
    }
    if (r < 16) {
        const int cq = r >> 2;
        if (wave >= 4) {
            const v4u pat = *(LAS v4u*)(ept + ((lane & 7) << 4)) & TM;
            const int un = ((h & 15) >> 1) ^ (lane >> 5);
            const v4u e4 = (*(LAS v4u*)(ept + un * 16) >> 8) & 0xffu;
            LAS unsigned char* ow = L + L_OB + wv * 272 + (((lane >> 3) & 1) * 64 + 32 * (g & 1) + 8 * (g >> 1) + (lane & 7)) * 2;
            int oo = WS_O + ((wv * 32 + h) * 32 + g) * 1024 + cq * 256 + i15 * 16;
            PIN(ow); PIN(oo);
            v4u m4;
            STAMP(8);
            SLEEP(ST_DELAY);
#pragma unroll 1
            for (int n = 0; n < SPIN; ++n) {
                FENCE();
                m4 = ld16(rs, WS_ST + ((wv * 32 + (lane & 31)) * 2 + (h >> 4)) * 128 + un * 16) ^ e4;
                const bool done = !__any(or4(m4) & 0xffu);
                if (done) break;
                __builtin_amdgcn_s_sleep(ST_SLEEP);
            }
            STAMP(14);
            SLEEP(T_O);
            v4u o[8];
#pragma unroll
            for (int i = 0; i < 8; ++i) o[i] = ld16(rs, oo + i * 4096);
            STAMP(9);
            const float mv = bitsf((h & 1) ? m4.z : m4.x), lv = bitsf((h & 1) ? m4.w : m4.y);
            float mx = mv;
            mx = fmaxf(mx, dpp<0xB1>(mx)); mx = fmaxf(mx, dpp<0x4E>(mx)); mx = fmaxf(mx, dpp<0x124>(mx)); mx = fmaxf(mx, dpp<0x128>(mx));
            mx = max16(mx);
            const float wgt = __builtin_amdgcn_exp2f(mv - mx);
            float tot = quad_sum(wgt * lv);
            tot += dpp<0x124>(tot); tot += dpp<0x128>(tot);
            tot = sum16(tot);
            if (lane < 32) *(LAS float*)(L + L_WT + wv * 128 + (lane & 3) * 32 + (lane >> 2) * 4) = wgt * __builtin_amdgcn_rcpf(tot);
            FENCE();
            f32x4 wa = *(LAS f32x4*)(L + L_WT + wv * 128 + g * 32), wb = *(LAS f32x4*)(L + L_WT + wv * 128 + g * 32 + 16);
            PIN(wa); PIN(wb);
            f32x4 lo, hi;
#pragma unroll 1
            for (int n = 0; n < SPIN; ++n) {
                unsigned bad = 0;
#pragma unroll
                for (int i = 0; i < 8; ++i) {
                    const float wi = i < 4 ? wa[i & 3] : wb[i & 3];
                    const v4u t = o[i] ^ pat;
                    bad |= or4(t);
#pragma unroll
                    for (int k = 0; k < 4; ++k) {
                        lo[k] = i ? __builtin_fmaf(mlo(t[k]), wi, lo[k]) : mlo(t[k]) * wi;
                        hi[k] = i ? __builtin_fmaf(mhi(t[k]), wi, hi[k]) : mhi(t[k]) * wi;
                    }
                }
                if (!__any(bad & TM)) break;
                FENCE();
#pragma unroll
                for (int i = 0; i < 8; ++i) o[i] = ld16(rs, oo + i * 4096);
            }
            STAMP(10);
            const float e0 = rs16(rs32(lo[0], lo[2]), rs32(lo[1], lo[3])), e1 = rs16(rs32(hi[0], hi[2]), rs32(hi[1], hi[3]));
            const unsigned ob2 = pk(e0, e1);
            *(LAS u16*)ow = (u16)ob2;
            *(LAS u16*)(ow + 32) = (u16)(ob2 >> 16);
            BAR();
        } else {
            const v4u ex = *(LAS v4u*)(ept + (((g & 1) * 4 + (lane & 3)) << 4));
            LAS unsigned char* orr = L + L_OB + (lane & 3) * 272 + g * 16;
            int dst = (lane & 12) ? OOB : WS_X1 + (h * 4 + cq) * 1024 + wv * 256 + g * 64 + (lane & 3) * 16;
            PIN(orr); PIN(dst);
#pragma unroll 1
            for (int n = 0; n < 64 * SPIN && flag[2] != 0x01010101u; ++n) {}
            FENCE();
            v4u a[8];
            slab_frags<4, 4, 8>(L + L_KV, lane, wv, a);
            BAR();
            f32x4 d0, d1;
            slab_gemv<4>(a, orr, d0, d1);
            st16(v4u{tagb(pk(d0[0], d0[1]), ex.x), tagb(pk(d0[2], d0[3]), ex.y), tagb(pk(d1[0], d1[1]), ex.z), tagb(pk(d1[2], d1[3]), ex.w)}, rs, dst);
        }
        STAMP(11);
        if (r < 8 && wave == 4) {
            const int m0 = w * 8 + ((lane >> 2) & 1) * 4, fb = lane & 3;
            const int xo = ((m0 >> 3) & 7) * 64 + fb * 16 + (m0 & 4) * 2;
            const v2u eh = *(LAS v2u*)(ept + (xo & 127)) & TM;
            const v4u e5 = (*(LAS v4u*)(ept + ((lane & 7) << 4)) >> 8) & 0xffu;
            v2u hp, xr = {0u, 0u};
            if (lane < 8) xr = *(const GAS v2u*)((const GAS u16*)p.x + fb * 512 + m0);
#pragma unroll 1
            for (int n = 0; n < SPIN; ++n) {
                FENCE();
                hp = ld8(rs, WS_X2 + ((lane >> 3) * 8 + (m0 >> 6)) * 512 + xo) ^ eh;
                if (!__any((hp.x | hp.y) & TM)) break;
                __builtin_amdgcn_s_sleep(8);
            }
            f32x4 hx = {blo(hp.x) + blo(xr.x), bhi(hp.x) + bhi(xr.x), blo(hp.y) + blo(xr.y), bhi(hp.y) + bhi(xr.y)};
            GAS u16* op = (GAS u16*)p.out + fb * 512 + m0 + g;
            PIN(hx); PIN(op);
            v4u d[6];
#pragma unroll 1
            for (int n = 0; n < SPIN; ++n) {
                FENCE();
#pragma unroll
                for (int i = 0; i < 6; ++i) d[i] = ld16(rs, (i < 5 || lane < 24) ? WS_HD + (w * 48 + i * 8) * 128 + lane * 16 : OOB);
                unsigned bad = 0;
#pragma unroll
                for (int i = 0; i < 6; ++i) { if (i < 5 || lane < 24) d[i] ^= e5; bad |= or4(d[i]); }
                if (!__any(bad & 0xffu)) break;
            }
            STAMP(12);
            const f32x4 s = ((hx + __builtin_bit_cast(f32x4, d[0])) + (__builtin_bit_cast(f32x4, d[1]) + __builtin_bit_cast(f32x4, d[2]))) +
                            ((__builtin_bit_cast(f32x4, d[3]) + __builtin_bit_cast(f32x4, d[4])) + __builtin_bit_cast(f32x4, d[5]));
            float z = rs16(rs32(s[0], s[2]), rs32(s[1], s[3]));
            z += dpp<0x128>(z);
            if (!(lane & 8)) *op = (u16)pk(z, z);
            if (w == 0 && lane < 32) st4(*(LAS unsigned*)(ept + lane * 4), rs, WS_EP + lane * 4);
        }
    } else if (r < 24) {
        const int ms = r - 16;
        VMWAIT(0);
        const int u = i15 >> 2, bb = i15 & 3;
        const int fo = ((lane >> 1) & 3) * 272 + (lane >> 3) * 32 + (lane & 1) * 16;
        const v4u pat = *(LAS v4u*)(ept + (((g & 1) * 4 + bb) << 4)) & TM;
        const v2u ef = *(LAS v2u*)(ept + i15 * 8);
        v4u a[8];
        blk_frags<2>(L + L_KV + wave * 12 * PP, lane, a);
        LAS unsigned char* rw = L + L_RED + wave * 1088 + bb * 272 + u * 64 + g * 16;
        LAS unsigned char* rr = L + L_RED + fo;
        int dst = WS_X2 + (xg * 8 + ms) * 512 + lane * 8;
        PIN(rw); PIN(rr); PIN(dst);
        STAMP(9);
        v4u c[2];
        gather_all<2>(rs, WS_X1 + ((xg * 4 + (wave >> 1)) * 4 + u) * 1024 + (wave & 1) * 512 + g * 64 + bb * 16, 0, 256, pat, TM, c);
        STAMP(10);
        f32x4 d[4];
#pragma unroll
        for (int t = 0; t < 4; ++t) d[t] = mfma(a[2 * t], c[0], f32x4{0.f, 0.f, 0.f, 0.f});
#pragma unroll
        for (int t = 0; t < 4; ++t) d[t] = mfma(a[2 * t + 1], c[1], d[t]);
        const f32x4 da = rs8x4(d[0], d[2]), db = rs8x4(d[1], d[3]);
        *(LAS f32x4*)rw = rs4x4(da, db);
        BAR();
        STAMP(15);
        if (wave == 0) {
            f32x4 v[8];
#pragma unroll
            for (int i = 0; i < 8; ++i) v[i] = *(LAS f32x4*)(rr + i * 1088);
            const f32x4 s = ((v[0] + v[1]) + (v[2] + v[3])) + ((v[4] + v[5]) + (v[6] + v[7]));
            st8(v2u{tagb(pk(s[0], s[1]), ef.x), tagb(pk(s[2], s[3]), ef.y)}, rs, dst);
        }
        STAMP(11);
    } else if (roleE) {
        const int e = ef_;
        const int u = i15 >> 2, bb = i15 & 3;
        const v4u e4 = *(LAS v4u*)(ept + (((g & 1) * 4 + bb) << 4));
        const v4u pat = e4 & TM;
        v4u etag = (e4 >> 8) & 0xffu;
        const v4u xf = *(const GAS v4u*)((const GAS u16*)p.x + bb * 512 + wave * 64 + (u >> 1) * 32 + g * 8);
        if (tid < 16) *(LAS unsigned*)(L + L_ACT + 320 + tid * 4) = 0u;
        VMWAIT(0);
        v4u a[8], dn[4];
        blk_frags<2>(L + L_KV + wave * 12 * PP, lane, a);
        {
            const v4u ones = {0x3f803f80u, 0x3f803f80u, 0x3f803f80u, 0x3f803f80u};
#pragma unroll
            for (int t = 0; t < 4; ++t) {
                f32x4 d = mfma(a[2 * t], ones, f32x4{0.f, 0.f, 0.f, 0.f});
                d = mfma(a[2 * t + 1], ones, d);
                if (i15 == 0) *(LAS f32x4*)(L + L_CS + wave * 256 + t * 64 + g * 16) = d;
            }
        }
        BAR();
        blk_frags<1>(L + L_KV + (wave * 12 + 8) * PP, lane, dn);
        float cs = 0.f;
#pragma unroll
        for (int i = 0; i < 8; ++i) cs += *(LAS float*)(L + L_CS + i * 256 + lane * 4);
        f32x4 xa = unp01(xf), xb = unp23(xf);
        LAS unsigned char* rw = L + L_RED + wave * 1088 + bb * 272 + g * 16;
        LAS unsigned char* sw = L + L_STAT + bb * 64 + wave * 8;
        LAS unsigned char* rr = L + L_RED + wv * 272 + lane * 4;
        LAS unsigned char* sr = L + L_STAT + wv * 64 + (lane & 7) * 8;
        LAS unsigned char* aw = L + L_ACT + wv * 80 + (lane & 31) * 2;
        LAS unsigned char* ar[4];
#pragma unroll
        for (int t = 0; t < 4; ++t) { ar[t] = L + L_ACT + (u == t ? bb * 80 : 320) + g * 16; PIN(ar[t]); PIN(etag[t]); PIN(xa[t]); PIN(xb[t]); }
        int dst = WS_HD + (((wave * 4 + u) * 2 + (g >> 1)) * 48 + e) * 128 + (g & 1) * 64 + bb * 16;
        PIN(rw); PIN(sw); PIN(rr); PIN(sr); PIN(aw); PIN(dst); PIN(cs);
        STAMP(9);
        v4u c[4];
        gather_all<4>(rs, WS_X2 + (u * 8 + wave) * 512 + g * 64 + bb * 16, 4 * 8 * 512, 256, pat, TM, c);
        STAMP(10);
        f32x4 za = rs8x4(unp01(c[0]) + unp01(c[2]), unp01(c[1]) + unp01(c[3])), zb = rs8x4(unp23(c[0]) + unp23(c[2]), unp23(c[1]) + unp23(c[3]));
        add4x8(za, zb);
        za += xa; zb += xb;
        const v4u h0 = {pk(za[0], za[1]), pk(za[2], za[3]), pk(zb[0], zb[1]), pk(zb[2], zb[3])};
        v4u h1;
#pragma unroll
        for (int k = 0; k < 4; ++k) h1[k] = __builtin_amdgcn_update_dpp(0u, h0[k], 0x128, 0xf, 0xf, true);
        f32x4 d[4];
#pragma unroll
        for (int t = 0; t < 4; ++t) d[t] = mfma(a[2 * t], h0, f32x4{0.f, 0.f, 0.f, 0.f});
#pragma unroll
        for (int t = 0; t < 4; ++t) d[t] = mfma(a[2 * t + 1], h1, d[t]);
        float s1 = 0.f, s2 = 0.f;
#pragma unroll
        for (int k = 0; k < 4; ++k) { s1 = dot2(h0[k], 0x3f803f80u, s1); s2 = dot2(h0[k], h0[k], s2); }
        s1 += dpp<0x128>(s1); s2 += dpp<0x128>(s2);
        s1 = sum32(sum16(s1)); s2 = sum32(sum16(s2));
        if (u == 0) {
            *(LAS f32x2*)sw = f32x2{s1, s2};
#pragma unroll
            for (int t = 0; t < 4; ++t) *(LAS f32x4*)(rw + t * 64) = d[t];
        }
        BAR();
        STAMP(15);
        if (wave < 4) {
            f32x2 st = *(LAS f32x2*)sr;
            float v[8];
#pragma unroll
            for (int i = 0; i < 8; ++i) v[i] = *(LAS float*)(rr + i * 1088);
            float t1 = quad_sum(st[0]), t2 = quad_sum(st[1]);
            t1 += dpp<0x124>(t1); t2 += dpp<0x124>(t2);
            const float mean = t1 * (1.f / 512.f);
            const float rstd = __builtin_amdgcn_rsqf(fmaxf(t2 * (1.f / 512.f) - mean * mean, 0.f) + 1e-5f);
            const float z = ((((v[0] + v[1]) + (v[2] + v[3])) + ((v[4] + v[5]) + (v[6] + v[7]))) - mean * cs) * rstd;
            const auto gu = __builtin_amdgcn_permlane32_swap(fbits(z), fbits(z), false, false);
            const float gt = bitsf(gu[0]), up = bitsf(gu[1]);
            const float act = gt * up * __builtin_amdgcn_rcpf(1.f + __builtin_amdgcn_exp2f(gt * -1.4426950408889634f));
            *(LAS u16*)aw = (u16)pk(act, act);
        }
        BAR();
        {
            v4u af[4];
#pragma unroll
            for (int t = 0; t < 4; ++t) af[t] = *(LAS v4u*)ar[t];
            f32x4 o = {0.f, 0.f, 0.f, 0.f};
#pragma unroll
            for (int t = 0; t < 4; ++t) o = mfma(dn[t], af[t], o);
            st16(((__builtin_bit_cast(v4u, o) + 0x80u) & ~0xffu) | etag, rs, dst);
        }
        STAMP(11);
    }
}
}

extern "C" void kernel_launch(void* const* d_in, const int* in_sizes, int n_in, void* d_out, int out_size, void* d_ws, size_t ws_size,
                              hipStream_t stream) {
    Params p;
    p.x = (const u16*)d_in[0]; p.ckv = (const u16*)d_in[1]; p.wq = (const u16*)d_in[2]; p.wuk = (const u16*)d_in[3];
    p.wuv = (const u16*)d_in[4]; p.wo = (const u16*)d_in[5]; p.wg = (const u16*)d_in[7]; p.wu = (const u16*)d_in[8]; p.wd = (const u16*)d_in[9];
    p.ws = (unsigned char*)d_ws; p.out = (u16*)d_out;
    (void)hipFuncSetAttribute((const void*)fused_decode, hipFuncAttributeMaxDynamicSharedMemorySize, LDS_BYTES);
    hipLaunchKernelGGL(fused_decode, dim3(256), dim3(512), LDS_BYTES, stream, p);
}
```

```cpp
#include <hip/hip_runtime.h>
#include <stdint.h>

#define GAS __attribute__((address_space(1)))
#define LAS __attribute__((address_space(3)))
typedef unsigned v4u __attribute__((ext_vector_type(4)));
typedef unsigned v2u __attribute__((ext_vector_type(2)));
typedef float f32x4 __attribute__((ext_vector_type(4)));
typedef float f32x2 __attribute__((ext_vector_type(2)));
typedef short s16x4 __attribute__((ext_vector_type(4)));
typedef short s16x8 __attribute__((ext_vector_type(8)));
typedef __bf16 bf16x2 __attribute__((ext_vector_type(2)));
typedef __bf16 bf16x8 __attribute__((ext_vector_type(8)));
typedef unsigned short u16;
typedef unsigned long long u64;
typedef __amdgpu_buffer_rsrc_t rsrc_t;
#define DI __device__ __forceinline__

namespace {
constexpr int NFF = 43;
constexpr int WS_EP = 0;
constexpr int WS_Q1 = 4096;
constexpr int WS_QA = WS_Q1 + 524288;
constexpr int WS_ST = WS_QA + 131072;
constexpr int WS_DB = WS_ST + 32768;
constexpr int WS_R2 = WS_DB + 131072;
constexpr int WS_DQ = WS_R2 + 8192;
constexpr int WS_X1 = WS_DQ + 16384;
constexpr int WS_X2 = WS_X1 + 131072;
constexpr int WS_HD = WS_X2 + 32768;
constexpr int WS_O = WS_HD + 64 * 48 * 128;
constexpr int WS_END = WS_O + (4 << 20);
constexpr int KVS = 1040;
constexpr int L_KV = 0, L_Q = 128 * KVS, L_P = 144 * KVS;
constexpr int L_EP = L_P + 8192, L_XN = L_EP + 128, L_SM = L_XN + 4 * 144, L_SS = L_SM + 512, L_FL = L_SS + 2048, LDS_BYTES = L_FL + 128;
constexpr int L_OB = L_Q, L_WT = L_Q + 2048, L_RED = L_Q + 4096, L_CS = L_Q + 4096 + 8704, L_STAT = L_CS + 2304, L_ACT = L_STAT + 256;
static_assert(L_ACT + 4 * 80 + 64 <= L_P, "tail scratch must fit the q rows");
static_assert(LDS_BYTES <= 163840, "lds");
constexpr unsigned TM = 0x00010001u;
constexpr int SPIN = 1 << 18;
constexpr unsigned CODE_BYTES = 23808;
constexpr int OOB = 0x7ffff000;
constexpr int T_HOLD = 95;
constexpr int T_Q1 = 19;
constexpr int T_DQ = 190;
constexpr int T_QA = 205;
constexpr int T_QS = 25, T_LAND = 80, T_O = 155, T_LATE = 25;
constexpr int T_W = 520, T_WE = 440;
constexpr int T_HD = 212;
constexpr int KV_THR = 8;

struct Params {
    const u16 *x, *ckv, *wq, *wuk, *wuv, *wo, *wg, *wu, *wd;
    unsigned char* ws;
    u16* out;
};

DI v4u ld16(rsrc_t rs, int off) { return __builtin_amdgcn_raw_buffer_load_b128(rs, off, 0, 16); }
DI v2u ld8(rsrc_t rs, int off) { return __builtin_amdgcn_raw_buffer_load_b64(rs, off, 0, 16); }
DI void st16(v4u v, rsrc_t rs, int off) { __builtin_amdgcn_raw_buffer_store_b128(v, rs, off, 0, 16); }
DI void st8(v2u v, rsrc_t rs, int off) { __builtin_amdgcn_raw_buffer_store_b64(v, rs, off, 0, 16); }
DI void st4(unsigned v, rsrc_t rs, int off) { __builtin_amdgcn_raw_buffer_store_b32(v, rs, off, 0, 16); }
#define BAR() do { asm volatile("s_waitcnt lgkmcnt(0)" ::: "memory"); __builtin_amdgcn_s_barrier(); asm volatile("" ::: "memory"); } while (0)
#define VMWAIT(n) asm volatile("s_waitcnt vmcnt(%0)" :: "i"(n) : "memory")
#define SPIN4(w) do { _Pragma("unroll 1") for (int n_ = 0; n_ < 64 * SPIN && flag[w] != 0x01010101u; ++n_) {} asm volatile("" ::: "memory"); } while (0)
#define SPIN8(o) do { _Pragma("unroll 1") for (int n_ = 0; n_ < 64 * SPIN && *(volatile LAS u64*)(fb8 + (o)) != 0x0101010101010101ull; ++n_) {} asm volatile("" ::: "memory"); } while (0)
#define FENCE() asm volatile("" ::: "memory")

DI void dma16(const void* gsrc, unsigned lds_dst) {
    unsigned keep;
    asm volatile("s_mov_b32 %0, m0\n\ts_mov_b32 m0, %2\n\ts_nop 0\n\tglobal_load_lds_dwordx4 %1, off nt\n\ts_mov_b32 m0, %0"
                 : "=&s"(keep) : "v"(gsrc), "s"(lds_dst) : "memory");
}
DI void dma16s(const void* sbase, unsigned voff, unsigned lds_dst) {
    asm volatile("s_mov_b32 m0, %2\n\ts_nop 3\n\tglobal_load_lds_dwordx4 %0, %1 nt" :: "v"(voff), "s"(sbase), "s"(lds_dst) : "memory");
}
DI f32x4 mfma(v4u a, v4u b, f32x4 c) {
    return __builtin_amdgcn_mfma_f32_16x16x32_bf16(__builtin_bit_cast(bf16x8, a), __builtin_bit_cast(bf16x8, b), c, 0, 0, 0);
}
#define MACC(acc, a, b) asm volatile("v_mfma_f32_16x16x32_bf16 %0, %1, %2, %0" : "+v"(acc) : "v"(a), "v"(b))
#define MNEW(acc, a, b) asm volatile("v_mfma_f32_16x16x32_bf16 %0, %1, %2, 0" : "=&v"(acc) : "v"(a), "v"(b))
DI float max3(float a, float b, float c) { float r; asm("v_max3_f32 %0, %1, %2, %3" : "=v"(r) : "v"(a), "v"(b), "v"(c)); return r; }
DI float max2(float a, float b) { float r; asm("v_max_f32 %0, %1, %2" : "=v"(r) : "v"(a), "v"(b)); return r; }
DI v2u trd(LAS unsigned char* p) { return __builtin_bit_cast(v2u, __builtin_amdgcn_ds_read_tr16_b64_v4i16((LAS s16x4*)p)); }
DI v4u trd2(LAS unsigned char* p, int second) { const v2u a = trd(p), b = trd(p + second); return v4u{a.x, a.y, b.x, b.y}; }
DI unsigned pk(float a, float b) { return __builtin_bit_cast(unsigned, __builtin_convertvector(f32x2{a, b}, bf16x2)); }
DI float blo(unsigned u) { return __builtin_bit_cast(float, u << 16); }
DI float bhi(unsigned u) { return __builtin_bit_cast(float, u & 0xffff0000u); }
DI float mlo(unsigned u) { return __builtin_bit_cast(float, (u << 16) | 0x8000u); }
DI float mhi(unsigned u) { return __builtin_bit_cast(float, u); }
DI unsigned fbits(float f) { return __builtin_bit_cast(unsigned, f); }
DI float bitsf(unsigned u) { return __builtin_bit_cast(float, u); }
DI unsigned tagb(float a, float b, unsigned e) { return (__builtin_amdgcn_perm(fbits(b) + 0x10000u, fbits(a) + 0x10000u, 0x07060302u) & ~TM) | (e & TM); }
DI unsigned tagq(float a, float b, unsigned e) { return (__builtin_amdgcn_perm(fbits(b) + 0x8000u, fbits(a) + 0x10000u, 0x07060302u) & ~1u) | (e & 1u); }
DI unsigned tagf(float f, unsigned e) { return ((fbits(f) + 0x80u) & ~0xffu) | ((e >> 8) & 0xffu); }
template <int CTRL> DI float dpp(float v) { return bitsf(__builtin_amdgcn_update_dpp(0, fbits(v), CTRL, 0xf, 0xf, true)); }
DI float quad_sum(float v) { v += dpp<0xB1>(v); v += dpp<0x4E>(v); return v; }
DI float col_sum(float v) { v += dpp<0x124>(v); v += dpp<0x128>(v); return v; }
DI float sum16(float v) { const auto r = __builtin_amdgcn_permlane16_swap(fbits(v), fbits(v), false, false); return bitsf(r[0]) + bitsf(r[1]); }
DI float sum32(float v) { const auto r = __builtin_amdgcn_permlane32_swap(fbits(v), fbits(v), false, false); return bitsf(r[0]) + bitsf(r[1]); }
DI float max16(float v) { const auto r = __builtin_amdgcn_permlane16_swap(fbits(v), fbits(v), false, false); return max2(bitsf(r[0]), bitsf(r[1])); }
DI float max32(float v) { const auto r = __builtin_amdgcn_permlane32_swap(fbits(v), fbits(v), false, false); return max2(bitsf(r[0]), bitsf(r[1])); }
DI float rs16(float a, float b) { const auto r = __builtin_amdgcn_permlane16_swap(fbits(a), fbits(b), false, false); return bitsf(r[0]) + bitsf(r[1]); }
DI float rs32(float a, float b) { const auto r = __builtin_amdgcn_permlane32_swap(fbits(a), fbits(b), false, false); return bitsf(r[0]) + bitsf(r[1]); }
DI float dot2(unsigned a, unsigned b, float c) { return __builtin_amdgcn_fdot2_f32_bf16(__builtin_bit_cast(bf16x2, a), __builtin_bit_cast(bf16x2, b), c, false); }
#define PIN(x) asm volatile("" : "+v"(x))
template <int NOP> DI f32x4 rs8x4(f32x4 a, f32x4 b) {
    f32x4 r;
    asm volatile("s_nop %12\n\t"
                 "v_add_f32_dpp %0, %4, %4 row_ror:8 row_mask:0xf bank_mask:0x3\n\tv_add_f32_dpp %1, %5, %5 row_ror:8 row_mask:0xf bank_mask:0x3\n\t"
                 "v_add_f32_dpp %2, %6, %6 row_ror:8 row_mask:0xf bank_mask:0x3\n\tv_add_f32_dpp %3, %7, %7 row_ror:8 row_mask:0xf bank_mask:0x3\n\t"
                 "v_add_f32_dpp %0, %8, %8 row_ror:8 row_mask:0xf bank_mask:0xc\n\tv_add_f32_dpp %1, %9, %9 row_ror:8 row_mask:0xf bank_mask:0xc\n\t"
                 "v_add_f32_dpp %2, %10, %10 row_ror:8 row_mask:0xf bank_mask:0xc\n\tv_add_f32_dpp %3, %11, %11 row_ror:8 row_mask:0xf bank_mask:0xc"
                 : "=&v"(r[0]), "=&v"(r[1]), "=&v"(r[2]), "=&v"(r[3])
                 : "v"(a[0]), "v"(a[1]), "v"(a[2]), "v"(a[3]), "v"(b[0]), "v"(b[1]), "v"(b[2]), "v"(b[3]), "i"(NOP));
    return r;
}
DI f32x4 rs4x4(f32x4 a, f32x4 b) {
    f32x4 r;
    asm volatile("s_nop 1\n\t"
                 "v_add_f32_dpp %0, %4, %4 row_shl:4 row_mask:0xf bank_mask:0x5\n\tv_add_f32_dpp %1, %5, %5 row_shl:4 row_mask:0xf bank_mask:0x5\n\t"
                 "v_add_f32_dpp %2, %6, %6 row_shl:4 row_mask:0xf bank_mask:0x5\n\tv_add_f32_dpp %3, %7, %7 row_shl:4 row_mask:0xf bank_mask:0x5\n\t"
                 "v_add_f32_dpp %0, %8, %8 row_shr:4 row_mask:0xf bank_mask:0xa\n\tv_add_f32_dpp %1, %9, %9 row_shr:4 row_mask:0xf bank_mask:0xa\n\t"
                 "v_add_f32_dpp %2, %10, %10 row_shr:4 row_mask:0xf bank_mask:0xa\n\tv_add_f32_dpp %3, %11, %11 row_shr:4 row_mask:0xf bank_mask:0xa"
                 : "=&v"(r[0]), "=&v"(r[1]), "=&v"(r[2]), "=&v"(r[3])
                 : "v"(a[0]), "v"(a[1]), "v"(a[2]), "v"(a[3]), "v"(b[0]), "v"(b[1]), "v"(b[2]), "v"(b[3]));
    return r;
}
DI void add4x8(f32x4& a, f32x4& b) {
    asm volatile("s_nop 1\n\t"
                 "v_add_f32_dpp %0, %0, %0 row_shl:4 row_mask:0xf bank_mask:0x5\n\tv_add_f32_dpp %1, %1, %1 row_shl:4 row_mask:0xf bank_mask:0x5\n\t"
                 "v_add_f32_dpp %2, %2, %2 row_shl:4 row_mask:0xf bank_mask:0x5\n\tv_add_f32_dpp %3, %3, %3 row_shl:4 row_mask:0xf bank_mask:0x5\n\t"
                 "v_add_f32_dpp %4, %4, %4 row_shl:4 row_mask:0xf bank_mask:0x5\n\tv_add_f32_dpp %5, %5, %5 row_shl:4 row_mask:0xf bank_mask:0x5\n\t"
                 "v_add_f32_dpp %6, %6, %6 row_shl:4 row_mask:0xf bank_mask:0x5\n\tv_add_f32_dpp %7, %7, %7 row_shl:4 row_mask:0xf bank_mask:0x5"
                 : "+v"(a[0]), "+v"(a[1]), "+v"(a[2]), "+v"(a[3]), "+v"(b[0]), "+v"(b[1]), "+v"(b[2]), "+v"(b[3]));
}
DI void colsum8(f32x4& a, f32x4& b) {
    asm volatile("s_nop 1\n\t"
                 "v_add_f32_dpp %0, %0, %0 row_ror:4 row_mask:0xf bank_mask:0xf\n\tv_add_f32_dpp %1, %1, %1 row_ror:4 row_mask:0xf bank_mask:0xf\n\t"
                 "v_add_f32_dpp %2, %2, %2 row_ror:4 row_mask:0xf bank_mask:0xf\n\tv_add_f32_dpp %3, %3, %3 row_ror:4 row_mask:0xf bank_mask:0xf\n\t"
                 "v_add_f32_dpp %4, %4, %4 row_ror:4 row_mask:0xf bank_mask:0xf\n\tv_add_f32_dpp %5, %5, %5 row_ror:4 row_mask:0xf bank_mask:0xf\n\t"
                 "v_add_f32_dpp %6, %6, %6 row_ror:4 row_mask:0xf bank_mask:0xf\n\tv_add_f32_dpp %7, %7, %7 row_ror:4 row_mask:0xf bank_mask:0xf\n\t"
                 "v_add_f32_dpp %0, %0, %0 row_ror:8 row_mask:0xf bank_mask:0xf\n\tv_add_f32_dpp %1, %1, %1 row_ror:8 row_mask:0xf bank_mask:0xf\n\t"
                 "v_add_f32_dpp %2, %2, %2 row_ror:8 row_mask:0xf bank_mask:0xf\n\tv_add_f32_dpp %3, %3, %3 row_ror:8 row_mask:0xf bank_mask:0xf\n\t"
                 "v_add_f32_dpp %4, %4, %4 row_ror:8 row_mask:0xf bank_mask:0xf\n\tv_add_f32_dpp %5, %5, %5 row_ror:8 row_mask:0xf bank_mask:0xf\n\t"
                 "v_add_f32_dpp %6, %6, %6 row_ror:8 row_mask:0xf bank_mask:0xf\n\tv_add_f32_dpp %7, %7, %7 row_ror:8 row_mask:0xf bank_mask:0xf"
                 : "+v"(a[0]), "+v"(a[1]), "+v"(a[2]), "+v"(a[3]), "+v"(b[0]), "+v"(b[1]), "+v"(b[2]), "+v"(b[3]));
}
DI f32x4 unp01(v4u v) { return f32x4{blo(v.x), bhi(v.x), blo(v.y), bhi(v.y)}; }
DI f32x4 unp23(v4u v) { return f32x4{blo(v.z), bhi(v.z), blo(v.w), bhi(v.w)}; }
DI unsigned or4(v4u v) { return v.x | v.y | v.z | v.w; }

template <int N, int SLEEP> DI void gather_masked(rsrc_t rs, int off, int stride, v4u pat, unsigned mask, v4u (&v)[N]) {
    unsigned need = (1u << N) - 1u;
#pragma unroll 1
    for (int n = 0; n < SPIN; ++n) {
        FENCE();
        v4u t[N];
#pragma unroll
        for (int i = 0; i < N; ++i) t[i] = ld16(rs, off + ((need >> i) & 1u ? i * stride : OOB / 2));
#pragma unroll
        for (int i = 0; i < N; ++i) {
            t[i] ^= pat;
            const bool take = (need >> i) & 1u;
            v[i] = take ? t[i] : v[i];
            if (!__any(or4(t[i]) & mask)) need &= ~(1u << i);
        }
        if (!need) break;
        if (SLEEP) __builtin_amdgcn_s_sleep(SLEEP);
    }
}
template <int N> DI void gather_all(rsrc_t rs, int off, int stride2, int stride1, v4u pat, unsigned mask, v4u (&v)[N]) {
#pragma unroll 1
    for (int n = 0; n < SPIN; ++n) {
        FENCE();
#pragma unroll
        for (int i = 0; i < N; ++i) v[i] = ld16(rs, off + (i >> 1) * stride2 + (i & 1) * stride1);
        unsigned bad = 0;
#pragma unroll
        for (int i = 0; i < N; ++i) { v[i] ^= pat; bad |= or4(v[i]); }
        if (!__any(bad & mask)) break;
        __builtin_amdgcn_s_sleep(1);
    }
}
DI unsigned slab_voff(int lane) { return (lane >> 4) * 8192 + (((lane & 15) ^ ((lane >> 4) << 2)) << 4); }
template <int NS, int GS, int SS, int SS2> DI void slab_frags(LAS unsigned char* img, int lane, int wv, v4u (&a)[2 * NS]) {
    const int g = lane >> 4, q = (lane >> 2) & 3, p = lane & 3;
    LAS unsigned char* b = img + ((g >> 1) * GS + 2 * (g & 1)) * KVS + q * 256 + ((4 * (wv ^ q) + p) << 4);
#pragma unroll
    for (int s = 0; s < NS; ++s)
#pragma unroll
        for (int t = 0; t < 2; ++t) a[2 * s + t] = trd2(b + ((s & 1) * SS + (s >> 1) * SS2) * KVS + t * 8, KVS);
}
template <int NS> DI void slab_gemv(const v4u (&a)[2 * NS], LAS unsigned char* bvec, f32x4& d0, f32x4& d1) {
    d0 = f32x4{0.f, 0.f, 0.f, 0.f}; d1 = d0;
    v4u bf[NS];
#pragma unroll
    for (int s = 0; s < NS; ++s) bf[s] = *(LAS v4u*)(bvec + s * 64);
    __builtin_amdgcn_sched_barrier(0);
#pragma unroll
    for (int s = 0; s < NS; ++s) {
        d0 = mfma(a[2 * s], bf[s], d0);
        d1 = mfma(a[2 * s + 1], bf[s], d1);
    }
}
DI void ring(rsrc_t rs, LAS unsigned char* ept, int off, int lane) {
    const v4u e = (*(LAS v4u*)ept >> 8) & 0xffu;
    st16(e | ((unsigned)__builtin_amdgcn_s_memrealtime() << 8), rs, lane == 0 ? off : OOB);
}
template <int SLP> DI unsigned due_time(rsrc_t rs, LAS unsigned char* ept, int off, int lead, v4u m) {
    const v4u e = (*(LAS v4u*)ept >> 8) & 0xffu;
#pragma unroll 1
    for (int n = 0; n < SPIN; ++n) {
        if (!__any(off != OOB && (or4(m ^ e) & 0xffu))) break;
        __builtin_amdgcn_s_sleep(SLP);
        FENCE();
        m = ld16(rs, off);
    }
    const unsigned now = (unsigned)__builtin_amdgcn_s_memrealtime();
    int d = off != OOB ? (int)(m.x - (now << 8)) >> 8 : -100000;
    d = max(d, __builtin_amdgcn_update_dpp(0, d, 0xB1, 0xf, 0xf, true)); d = max(d, __builtin_amdgcn_update_dpp(0, d, 0x4E, 0xf, 0xf, true));
    d = max(d, __builtin_amdgcn_update_dpp(0, d, 0x124, 0xf, 0xf, true)); d = max(d, __builtin_amdgcn_update_dpp(0, d, 0x128, 0xf, 0xf, true));
    return now + (unsigned)min(max(max(__builtin_amdgcn_readlane(d, 0), __builtin_amdgcn_readlane(d, 16)), max(__builtin_amdgcn_readlane(d, 32), __builtin_amdgcn_readlane(d, 48))) + lead, 400);
}
DI void until(unsigned t) { while (t - (unsigned)__builtin_amdgcn_s_memrealtime() - 1u < 2000u) {} }
DI float rbf(float f) { return blo(pk(f, f)); }
constexpr int PP = 1088, L_DN = 64 * PP;
DI int blk_chunk(int lane) { return (lane & 7) ^ ((lane >> 3) & 2); }
template <int NS, int PIECE> DI void blk_frags(LAS unsigned char* img, int lane, v4u (&a)[4 * NS]) {
    const int g = lane >> 4, q = (lane >> 2) & 3, p = lane & 3;
    LAS unsigned char* b = img + g * PIECE + q * 128 + p * 8;
#pragma unroll
    for (int t = 0; t < 4; ++t)
#pragma unroll
        for (int s = 0; s < NS; ++s) a[t * NS + s] = trd2(b + ((t ^ (q >> 1)) << 5) + s * 4 * PIECE, 512);
}


__global__ void __launch_bounds__(512, 2) fused_decode(Params p) {
    extern __shared__ __attribute__((aligned(16))) unsigned char smem[];
    LAS unsigned char* L = (LAS unsigned char*)smem;
    const unsigned lbase = (unsigned)(size_t)L;
    const int tid = threadIdx.x, lane = tid & 63, wave = __builtin_amdgcn_readfirstlane(tid >> 6), w = blockIdx.x;
    if (tid >= 64 && tid < 96) *(LAS unsigned*)(L + L_FL + (tid - 64) * 4) = 0u;
    if (wave == 0) {
        u64 pc;
        asm volatile("s_getpc_b64 %0" : "=s"(pc));
        const unsigned co = min(((((unsigned)w >> 3) & 15u) * 16u + (lane & 15)) * 128u, CODE_BYTES - 128u);
        asm volatile("s_mov_b32 m0, %2\n\ts_nop 3\n\tglobal_load_lds_dword %0, %1" :: "v"(co), "s"(pc & ~127ull), "s"(lbase + L_P + 4352) : "memory");
    }
    const rsrc_t rs = __builtin_amdgcn_make_buffer_rsrc(p.ws, 0, WS_END, 0x00020000);
    const int xg = w & 7, r = w >> 3;
    const int h = xg * 4 + (r & 3), j8 = r >> 2;
    const int b = xg >> 1, ks = (xg & 1) * 16 + (r & 15);
    const int i15 = lane & 15, g = lane >> 4;
    LAS unsigned char* ept = L + L_EP;
    volatile LAS unsigned* flag = (volatile LAS unsigned*)(L + L_FL);
    const int wv = wave & 3;
    const unsigned char* kv = (const unsigned char*)p.ckv + ((size_t)b * 4096 + ks * 128 + wv * 32) * 1024;
    const unsigned kvl = lbase + L_KV + wv * 32 * KVS;

    volatile LAS unsigned char* fb8 = (volatile LAS unsigned char*)flag;
    if (wave < 4) asm volatile("s_mov_b32 m0, %2\n\ts_nop 3\n\tglobal_load_lds_dwordx4 %0, %1" :: "v"(tid * 16), "s"(p.x), "s"(lbase + L_P + wave * 1024) : "memory");
    if (wave == 2) asm volatile("s_mov_b32 m0, %2\n\ts_nop 3\n\tglobal_load_lds_dword %0, %1 sc1" :: "v"(WS_EP + (lane & 31) * 4), "s"(p.ws), "s"(lbase + L_P + 4096) : "memory");
    const size_t so = (size_t)(j8 * 64) * 8192 + h * 256;
    const unsigned vo = slab_voff(lane);
    {
        const unsigned char* src = (const unsigned char*)p.wq + so + wave * 65536;
        const unsigned dst = lbase + L_KV + (32 * (wave >> 1) + 28 + 2 * (wave & 1)) * KVS;
        dma16s(src, vo, dst);
        dma16s(src + 32768, vo, dst + KVS);
    }
    BAR();
    const bool isT = r < 16;
    const unsigned tstart = (unsigned)__builtin_amdgcn_s_memrealtime();
    if (__builtin_expect(wave >= 4, 1)) {
        const unsigned char* src = isT ? kv : (const unsigned char*)p.wuk + (size_t)((j8 & 3) * 128 + wv * 32) * 8192 + h * 256;
        const int sstr = isT ? 1024 : 32768, n = isT ? 16 + KV_THR : 8;
        const unsigned svo = isT ? lane * 16 : vo, dst = kvl + (isT ? 0 : 20 * KVS);
        if (isT) { VMWAIT(0); fb8[16 + wave] = 1; until(tstart + T_HOLD); } else __builtin_amdgcn_s_sleep(2);
#pragma unroll 1
        for (int i = 0; i < n; ++i) {
            dma16s(src + i * sstr, svo, dst + i * KVS);
            if (i == 4) { VMWAIT(4 + 1); fb8[16 + wave] = 1; }
            VMWAIT(KV_THR);
        }
    }
    int wave1 = wave;
    asm volatile("" : "+s"(wave1));
    if (__builtin_expect(wave1 < 4, 1)) {
        VMWAIT(2);
        {
            const v4u xv = *(LAS v4u*)(L + L_P + tid * 16);
            const unsigned ep = *(LAS unsigned*)(L + L_P + 4096 + lane * 4);
            float s1 = 0.f, s2 = 0.f;
#pragma unroll
            for (int k = 0; k < 4; ++k) { s1 = dot2(xv[k], 0x3f803f80u, s1); s2 = dot2(xv[k], xv[k], s2); }
            s1 = sum32(sum16(col_sum(quad_sum(s1)))); s2 = sum32(sum16(col_sum(quad_sum(s2))));
            const float mean = s1 * (1.f / 512.f);
            const float var = fmaxf(s2 * (1.f / 512.f) - mean * mean, 0.f);
            const float mb = rbf(mean), rstd = __builtin_amdgcn_rcpf(rbf(__builtin_amdgcn_sqrtf(rbf(rbf(var) + 1e-5f))));
            v4u xn;
#pragma unroll
            for (int k = 0; k < 4; ++k) { const unsigned c = pk(blo(xv[k]) - mb, bhi(xv[k]) - mb); xn[k] = pk(blo(c) * rstd, bhi(c) * rstd); }
            if ((lane >> 3) == j8) *(LAS v4u*)(L + L_XN + wave * 144 + (lane & 7) * 16) = xn;
            const unsigned t = ((ep & 1u) | ((ep >> 15) & 2u)) + 1u;
            if (wave == 2) *(LAS unsigned*)(ept + (lane & 31) * 4) = (t & 1u) | ((t & 2u) << 15) | ((ep + 0x100u) & 0xff00u);
        }
        int q1dst = (lane & 8) ? OOB : WS_Q1 + (h * 8 + j8) * 2048 + wave * 512 + (lane & 4) * 64 + g * 64 + (lane & 3) * 16;
        PIN(q1dst);
        VMWAIT(0);
        fb8[16 + wave] = 1;
#pragma unroll 1
        for (int n = 0; n < 64 * SPIN && *(volatile LAS u64*)(fb8 + 16) != 0x0101010101010101ull; ++n) {}
        FENCE();
        {
            v4u a[4];
            slab_frags<2, 32, 64, 0>(L + L_KV + 28 * KVS, lane, wave, a);
            const v4u e = *(LAS v4u*)(ept + (((g & 1) * 4 + (lane & 3)) << 4));
            f32x4 d0, d1;
            slab_gemv<2>(a, L + L_XN + (lane & 3) * 144 + g * 16, d0, d1);
            const f32x4 d = (lane & 4) ? d1 : d0;
            st16(v4u{tagf(d[0], e.x), tagf(d[1], e.y), tagf(d[2], e.z), tagf(d[3], e.w)}, rs, q1dst);
            FENCE();
            fb8[12 + wave] = 1;
            if (wave == 3 && (r & 7) == 7) ring(rs, ept, WS_DQ + (xg * 4 + (r >> 3)) * 128, lane);
        }
    }
    int wave2 = wave;
    asm volatile("" : "+s"(wave2));
    const int ef_ = xg * 6 + (r - 24);
    const bool roleE = r >= 24 && r < 30 && ef_ < NFF;
    if (!isT) {
        if (wave2 >= 4) {
            VMWAIT(0);
            fb8[24 + wv] = 1;
        } else {
            v4u q[4];
            const int u = i15 >> 2, bb = i15 & 3;
            v4u uk[8];
            LAS unsigned char* ukr = L + L_KV + (32 * (u >> 1) + 20 + 4 * (u & 1)) * KVS + bb * 256 + ((4 * (wave ^ bb) + g) << 4);
            const v4u pa = (*(LAS v4u*)(ept + (((g & 1) * 4 + bb) << 4)) >> 8) & 0xffu;
            const v2u e2 = *(LAS v2u*)(ept + i15 * 8);
            LAS unsigned char* rw = L + L_P + wave * 2048 + bb * 256 + g * 64 + u * 16;
            int qsrc = WS_Q1 + (h * 8 + 2 * u) * 2048 + wave * 512 + g * 64 + bb * 16;
            int qdst = WS_QA + (g * 32 + h) * 1024 + (j8 & 3) * 256 + wave * 128 + i15 * 8;
            PIN(rw); PIN(qdst); PIN(qsrc);
            __builtin_amdgcn_s_sleep(T_Q1);
#pragma unroll
            for (int i = 0; i < 4; ++i) q[i] = ld16(rs, qsrc + (i >> 1) * 2048 + (i & 1) * 256);
            SPIN4(6);
#pragma unroll
            for (int t = 0; t < 8; ++t) uk[t] = *(LAS v4u*)(ukr + ((t >> 2) * 64 + (t & 3)) * KVS);
#pragma unroll 1
            for (int n = 0; n < SPIN; ++n) {
                q[0] ^= pa; q[1] ^= pa; q[2] ^= pa; q[3] ^= pa;
                if (!__any((or4(q[0]) | or4(q[1]) | or4(q[2]) | or4(q[3])) & 0xffu)) break;
                FENCE();
#pragma unroll
                for (int i = 0; i < 4; ++i) q[i] = ld16(rs, qsrc + (i >> 1) * 2048 + (i & 1) * 256);
            }
            f32x4 qa = __builtin_bit_cast(f32x4, q[0]) + __builtin_bit_cast(f32x4, q[2]), qb = __builtin_bit_cast(f32x4, q[1]) + __builtin_bit_cast(f32x4, q[3]);
            colsum8(qa, qb);
            const v4u qf = {pk(qa[0], qa[1]), pk(qa[2], qa[3]), pk(qb[0], qb[1]), pk(qb[2], qb[3])};
            f32x4 d0 = {0.f, 0.f, 0.f, 0.f}, d1 = d0;
#pragma unroll
            for (int t = 0; t < 4; ++t) {
                const v4u qm = u == t ? qf : v4u{0u, 0u, 0u, 0u};
                d0 = mfma(uk[t], qm, d0);
                d1 = mfma(uk[4 + t], qm, d1);
            }
            *(LAS f32x4*)rw = d0;
            *(LAS f32x4*)(rw + 1024) = d1;
            FENCE();
            fb8[wave] = 1;
            if (wave < 2) {
                SPIN4(0);
                f32x4 o = *(LAS f32x4*)(L + L_P + wave * 1024 + lane * 16);
#pragma unroll
                for (int i = 1; i < 4; ++i) o += *(LAS f32x4*)(L + L_P + i * 2048 + wave * 1024 + lane * 16);
                o *= 0.08838834764831845f * 1.4426950408889634f;
                st8(v2u{tagq(o[0], o[1], e2.x), tagq(o[2], o[3], e2.y)}, rs, qdst);
            }
        }
        BAR();
        until(tstart + (r < 24 ? T_W : T_WE));
        if (r < 24 || roleE) {
            const GAS unsigned char* gs = r < 24 ? (const GAS unsigned char*)p.wo + (size_t)(xg * 512 + wave * 64 + (lane >> 3)) * 1024 + (r - 16) * 128 + blk_chunk(lane) * 16
                : (const GAS unsigned char*)((lane & 4) ? p.wu : p.wg) + (size_t)(wave * 64 + (lane >> 3)) * 2752 + ef_ * 64 + (blk_chunk(lane) & 3) * 16;
            const int st = r < 24 ? 8192 : 8 * 2752;
#pragma unroll 1
            for (int i = 0; i < 8; ++i) dma16((const void*)(gs + (size_t)(i * st)), lbase + L_KV + (wave * 8 + i) * PP);
        }
        if (roleE) {
#pragma unroll 1
            for (int i = 0; i < 4; ++i)
                dma16s((const unsigned char*)p.wd + (size_t)(ef_ * 32 + i * 8) * 1024 + wave * 128, (lane >> 3) * 1024 + blk_chunk(lane) * 16, lbase + L_KV + L_DN + (wave * 4 + i) * PP);
        }
    } else if (wave2 >= 4) {
        LAS unsigned char* ka = L + L_KV + (32 * wv + 8 * ((i15 >> 2) & 1) + 2 * (i15 & 3) + (i15 >> 3)) * KVS + g * 256;
        fb8[40 + wv] = 1;
        v4u qf[32], fr[16];
#pragma unroll
        for (int s = 0; s < 16; ++s) fr[s] = *(LAS v4u*)(ka + s * 16);
        if (lane < 32) *(LAS unsigned*)(L + L_SM + lane * 16 + wv * 4) = 0xffffffffu;
        FENCE();
        fb8[48 + wv] = 1;
        SPIN4(3);
#pragma unroll 1
        for (int i = 16 + KV_THR; i < 32; ++i) { dma16s(kv + i * 1024, lane * 16, kvl + i * KVS); VMWAIT(KV_THR); }
        flag[24 + wv] = ((unsigned)__builtin_amdgcn_s_memrealtime() + T_LAND) | 1u;
        SPIN8(48);
#pragma unroll 1
        for (int i = 0; i < 8; ++i)
            dma16s((const unsigned char*)p.wuv + (size_t)(j8 * 128 + wv * 32 + i * 4) * 8192 + h * 256, vo, lbase + L_KV + (32 + (wv >> 1) * 32 + (wv & 1) * 8 + i) * KVS);
        LAS unsigned char* qa = L + L_Q + i15 * KVS + g * 256;
        LAS unsigned char* qb = L + L_KV + i15 * KVS + g * 256;
        LAS unsigned char* smw = L + L_SM + i15 * 16;
        LAS unsigned char* pw = L + L_P + wv * 1024 + lane * 16;
        LAS unsigned char* ssw = L + L_SS + lane * 16 + wv * 4;
        int dbo = lane < 32 ? WS_DB + xg * 16384 + (wv * 32 + lane) * 128 : OOB;
        PIN(smw); PIN(pw); PIN(ssw); PIN(dbo); PIN(qa); PIN(qb); PIN(ka);
        volatile LAS unsigned char* pf0 = fb8 + 68 + wv;
        PIN(pf0);
        f32x4 s0, s1, t0, t1;
        SPIN4(9);
        __builtin_amdgcn_s_setprio(1);
#pragma unroll
        for (int s = 0; s < 8; ++s) qf[s] = *(LAS v4u*)(((s & 1) ? qb : qa) + (s >> 1) * 16);
        __builtin_amdgcn_sched_barrier(0);
#pragma unroll
        for (int s = 0; s < 24; ++s) {
            if (s == 0) MNEW(s0, fr[0], qf[0]); else if (s == 1) MNEW(t0, fr[0], qf[1]); else if (s & 1) MACC(t0, fr[s >> 1], qf[s]); else MACC(s0, fr[s >> 1], qf[s]);
            qf[s + 8] = *(LAS v4u*)(((s & 1) ? qb : qa) + ((s + 8) >> 1) * 16);
            __builtin_amdgcn_sched_barrier(0);
        }
        VMWAIT(8);
        fb8[60 + wv] = 1;
#pragma unroll
        for (int s = 24; s < 32; ++s) {
            fr[s - 24] = *(LAS v4u*)(ka + 16 * KVS + (s - 24) * 16);
            if (s & 1) MACC(t0, fr[s >> 1], qf[s]); else MACC(s0, fr[s >> 1], qf[s]);
            __builtin_amdgcn_sched_barrier(0);
        }
#pragma unroll
        for (int s = 0; s < 16; ++s) {
            if (s < 8) fr[s + 8] = *(LAS v4u*)(ka + 16 * KVS + (s + 8) * 16);
            if (s == 0) MNEW(s1, fr[0], qf[0]); else MACC(s1, fr[s], qf[2 * s]);
            __builtin_amdgcn_sched_barrier(0);
        }
#define SB() __builtin_amdgcn_sched_barrier(0)
#define T1(s) do { MACC(t1, fr[s], qf[2 * (s) + 1]); SB(); } while (0)
        {
            float ma = max3(s0[0], s0[1], s0[2]);
            MNEW(t1, fr[0], qf[1]); SB();
            ma = max2(ma, s0[3]); SB();
            T1(1); T1(2);
            asm volatile("" : "+v"(s1));
            ma = max3(max3(ma, s1[0], s1[1]), s1[2], s1[3]); SB();
            T1(3);
            ma = max16(ma); SB();
            T1(4);
            ma = max32(ma);
            *(volatile LAS unsigned*)(smw + wv * 4) = fbits(ma) & ~0xffu; SB();
            T1(5);
            float mb = max2(max3(t0[0], t0[1], t0[2]), t0[3]); SB();
            T1(6); T1(7); T1(8);
            v4u m0 = *(volatile LAS v4u*)smw; SB();
            T1(9); T1(10); T1(11); T1(12); T1(13);
            if (__any(or4(m0) & 1u)) {
#pragma unroll 1
                for (int n = 0; n < 64 * SPIN && __any(or4(m0) & 1u); ++n) m0 = *(volatile LAS v4u*)smw;
            }
            T1(14);
            float m = max3(bitsf(m0.x), bitsf(m0.y), max2(bitsf(m0.z), bitsf(m0.w))); SB();
            T1(15);
#pragma unroll
            for (int k = 0; k < 4; ++k) { s0[k] -= m; s1[k] -= m; }
            SB();
#pragma unroll
            for (int k = 0; k < 4; ++k) s0[k] = __builtin_amdgcn_exp2f(s0[k]);
            SB();
            asm volatile("" : "+v"(t1));
            mb = max16(max3(max3(mb, t1[0], t1[1]), t1[2], t1[3]));
#pragma unroll
            for (int k = 0; k < 4; ++k) s1[k] = __builtin_amdgcn_exp2f(s1[k]);
            mb = max32(mb);
            *(volatile LAS unsigned*)(smw + 256 + wv * 4) = fbits(mb) & ~0xffu;
            *(LAS v4u*)pw = v4u{pk(s0[0], s0[1]), pk(s0[2], s0[3]), pk(s1[0], s1[1]), pk(s1[2], s1[3])};
            FENCE();
            *pf0 = 1;
            SB();
            s0 += s1;
            *(LAS float*)ssw = (s0[0] + s0[1]) + (s0[2] + s0[3]);
            m0 = *(volatile LAS v4u*)(smw + 256);
            if (__any(or4(m0) & 1u)) {
#pragma unroll 1
                for (int n = 0; n < 64 * SPIN && __any(or4(m0) & 1u); ++n) m0 = *(volatile LAS v4u*)(smw + 256);
            }
            m = max3(bitsf(m0.x), bitsf(m0.y), max2(bitsf(m0.z), bitsf(m0.w)));
#pragma unroll
            for (int k = 0; k < 4; ++k) { t0[k] = __builtin_amdgcn_exp2f(t0[k] - m); t1[k] = __builtin_amdgcn_exp2f(t1[k] - m); }
            *(LAS v4u*)(pw + 4096) = v4u{pk(t0[0], t0[1]), pk(t0[2], t0[3]), pk(t1[0], t1[1]), pk(t1[2], t1[3])};
            FENCE();
            pf0[4] = 1;
            t0 += t1;
            *(LAS float*)(ssw + 1024) = (t0[0] + t0[1]) + (t0[2] + t0[3]);
            FENCE();
            pf0[8] = 1;
        }
        if (wv < 2) {
            SPIN4(19);
            const v4u m0 = *(LAS v4u*)(smw + wv * 256);
            const f32x4 l0 = *(LAS f32x4*)(L + L_SS + wv * 1024 + lane * 16);
            const v2u e2 = (*(LAS v2u*)(ept + i15 * 8) >> 8) & 0xffu;
            const float l = sum32(sum16((l0[0] + l0[1]) + (l0[2] + l0[3])));
            st8(v2u{fbits(max3(bitsf(m0.x), bitsf(m0.y), max2(bitsf(m0.z), bitsf(m0.w)))) | e2.x, ((fbits(l) + 0x80u) & ~0xffu) | e2.y}, rs,
                lane < 16 ? WS_ST + (b * 32 + ks) * 256 + wv * 128 + lane * 8 : OOB);
        }
        __builtin_amdgcn_s_setprio(0);
        __builtin_amdgcn_s_sleep(4);
        flag[20 + wv] = due_time<4>(rs, ept, dbo, 0, ld16(rs, dbo)) | 1u;
        fb8[8 + wv] = 1;
    } else {
        if (wave == 2) {
            const int dq = lane < 32 ? WS_DQ + lane * 128 : OOB;
            until(tstart + T_DQ);
            flag[1] = due_time<8>(rs, ept, dq, T_QA, ld16(rs, dq)) | 1u;
        }
        LAS unsigned char* va = L + L_KV + (8 * (g & 1) + 2 * (i15 >> 2) + (g >> 1)) * KVS + wave * 256 + (lane & 3) * 8;
        LAS unsigned char* vb = va + 64 * KVS;
        LAS unsigned char* pr = L + L_P + lane * 16;
        LAS unsigned char* qw = L + wave * KVS + lane * 16;
        LAS unsigned char* qw0 = qw + L_Q;
        v4u e4 = *(LAS v4u*)(ept + ((lane & 7) << 4));
        v4u ed = (*(LAS v4u*)ept >> 8) & 0xffu;
        int ob = WS_O + ((b * 32 + 4 * g + (i15 >> 3)) * 32 + ks) * 1024 + wave * 256 + (lane & 7) * 16;
        int ddst = tid < 8 ? WS_DB + tid * 16384 + (b * 32 + ks) * 128 : OOB;
        int qsrc = WS_QA + (b * 32 + wave) * 1024 + lane * 16;
        PIN(ddst); PIN(va); PIN(vb); PIN(pr); PIN(ob); PIN(e4); PIN(ed); PIN(qw); PIN(qw0); PIN(qsrc);
        v2u vlo[32], vhi[32];
        {
            v4u q[8];
            bool asked = false;
#pragma unroll 1
            for (int n = 0; n < 64 * SPIN; ++n) {
                const unsigned due = __builtin_amdgcn_readfirstlane(flag[1]);
                asked = due != 0 && (int)((unsigned)__builtin_amdgcn_s_memrealtime() - due) >= 0;
                if (asked || __builtin_amdgcn_readfirstlane(flag[10]) == 0x01010101u) break;
            }
#pragma unroll
            for (int i = 0; i < 8; ++i) { q[i] = ld16(rs, asked ? qsrc + i * 4096 : OOB); __builtin_amdgcn_sched_barrier(0); }
            SPIN4(10);
#pragma unroll
            for (int i = 0; i < 32; ++i) vlo[i] = trd((i < 16 ? va : vb) + ((i >> 3) & 1) * 32 * KVS + (i & 7) * 32);
            FENCE();
            fb8[52 + wave] = 1;
            if (!asked) {
#pragma unroll 1
                for (int n = 0; n < 64 * SPIN && flag[1] == 0; ++n) {}
                until(__builtin_amdgcn_readfirstlane(flag[1]));
#pragma unroll
                for (int i = 0; i < 8; ++i) { q[i] = ld16(rs, qsrc + i * 4096); __builtin_amdgcn_sched_barrier(0); }
            }
            SPIN8(48);
            const v4u pq = e4 & 1u;
            unsigned bad = 0;
#pragma unroll
            for (int i = 0; i < 8; ++i) { q[i] ^= pq; bad |= or4(q[i]); *(LAS v4u*)((i < 4 ? qw0 : qw) + (i & 3) * 4 * KVS) = q[i]; __builtin_amdgcn_sched_barrier(0); }
            if (__any(bad & 1u)) {
                gather_masked<8, 6>(rs, qsrc, 4096, pq, 1u, q);
#pragma unroll
                for (int i = 0; i < 8; ++i) *(LAS v4u*)((i < 4 ? qw0 : qw) + (i & 3) * 4 * KVS) = q[i];
            }
            FENCE();
            fb8[36 + wave] = 1;
        }
        if (wave == 0) {
            SPIN4(9);
            const unsigned now = (unsigned)__builtin_amdgcn_s_memrealtime() + T_QS;
            v4u t;
#pragma unroll 1
            for (int n = 0; n < 64 * SPIN; ++n) {
                FENCE();
                t = *(LAS v4u*)(L + L_FL + 96);
                if (t.x & t.y & t.z & t.w & 1u) break;
                __builtin_amdgcn_s_sleep(1);
            }
            const int d = max(max(max((int)(t.x - now), (int)(t.y - now)), max((int)(t.z - now), (int)(t.w - now))), 0);
            st16(ed | ((now + d) << 8), rs, ddst);
        }
#pragma unroll 1
        for (int n = 0; n < 64 * SPIN && flag[15] != 0x01010101u; ++n) __builtin_amdgcn_s_sleep(1);
        FENCE();
#pragma unroll
        for (int i = 0; i < 32; ++i) vhi[i] = trd((i < 16 ? va : vb) + (((i >> 3) & 1) * 32 + 16) * KVS + (i & 7) * 32);
        v4u fr[32];
#pragma unroll
        for (int i = 0; i < 32; ++i) fr[i] = v4u{vlo[i].x, vlo[i].y, vhi[i].x, vhi[i].y};
        v4u pf[8];
#pragma unroll 1
        for (int n = 0; n < 64 * SPIN && flag[17] == 0; ++n) __builtin_amdgcn_s_sleep(1);
        f32x4 o[2][4];
        auto put = [&](int grp, int k) {
            const f32x4* q4 = o[grp & 1];
            const unsigned a0 = pk(q4[0][k], q4[1][k]), a1 = pk(q4[2][k], q4[3][k]), b0 = pk(q4[0][k + 1], q4[1][k + 1]), b1 = pk(q4[2][k + 1], q4[3][k + 1]);
            v4u u;
            u.x = __builtin_amdgcn_update_dpp(a0, b0, 0x128, 0xf, 0xc, false);
            u.y = __builtin_amdgcn_update_dpp(a1, b1, 0x128, 0xf, 0xc, false);
            u.z = __builtin_amdgcn_update_dpp(b0, a0, 0x128, 0xf, 0x3, false);
            u.w = __builtin_amdgcn_update_dpp(b1, a1, 0x128, 0xf, 0x3, false);
            __builtin_amdgcn_raw_buffer_store_b128((u & ~TM) | (e4 & TM), rs, ob + (grp & 1) * 128, ((grp >> 1) * 16 + k) * 32768, 16);
        };
        auto getp = [&](int j) {
            const unsigned f = flag[17 + j];
            FENCE();
#pragma unroll
            for (int t = 0; t < 4; ++t) pf[j * 4 + t] = *(LAS v4u*)(pr + (j * 4 + t) * 1024);
            FENCE();
            return f;
        };
        unsigned f1 = 0;
#pragma unroll
        for (int grp = 0; grp < 4; ++grp) {
            if (grp == 0 || (grp == 2 && __builtin_amdgcn_readfirstlane(f1) != 0x01010101u)) {
#pragma unroll 1
                for (int n = 0; n < 64 * SPIN; ++n)
                    if (__builtin_amdgcn_readfirstlane(getp(grp >> 1)) == 0x01010101u) break;
            }
#pragma unroll
            for (int t = 0; t < 4; t += 2) {
                if (grp == 1 && t == 2) f1 = getp(1);
                const v4u* pg = pf + (grp >> 1) * 4;
#pragma unroll
                for (int c = 0; c < 4; ++c) o[grp & 1][c] = mfma(pg[t], fr[t * 8 + (grp & 1) * 4 + c], t ? o[grp & 1][c] : f32x4{0.f, 0.f, 0.f, 0.f});
#pragma unroll
                for (int c = 0; c < 4; ++c) o[grp & 1][c] = mfma(pg[t + 1], fr[t * 8 + 8 + (grp & 1) * 4 + c], o[grp & 1][c]);
                if (grp) put(grp - 1, t);
#pragma unroll
                for (int i = 0; i < 8; ++i) { __builtin_amdgcn_sched_group_barrier(0x8, 1, 0); __builtin_amdgcn_sched_group_barrier(0x2, 2, 0); }
                __builtin_amdgcn_sched_barrier(0);
            }
        }
        put(3, 0);
        put(3, 2);
    }
    if (r < 16) {
        const int cq = r >> 2;
        {
            const int ln = wave >> 2, pj = lane >> 3;
            const v4u pat = *(LAS v4u*)(ept + ((lane & 7) << 4)) & TM;
            const int un = ((h & 15) >> 1) ^ (lane >> 5);
            const v4u e4 = (*(LAS v4u*)(ept + un * 16) >> 8) & 0xffu;
            const v4u ex = *(LAS v4u*)(ept + (((g & 1) * 4 + (lane & 3)) << 4));
            LAS unsigned char* ow = L + L_OB + wv * 272 + (ln * 64 + 16 * (pj & 3) + 8 * (pj >> 2) + (lane & 7)) * 2;
            LAS unsigned char* orr = L + L_OB + (lane & 3) * 272 + g * 16;
            LAS unsigned char* ww = L + L_WT + wave * 256 + (lane & 7) * 16 + ((lane >> 3) & 3) * 4 + (lane & 32) * 4;
            LAS unsigned char* wr = L + L_WT + wave * 256 + pj * 16;
            int oo = WS_O + ((wv * 32 + h) * 32 + pj) * 1024 + cq * 256 + ln * 128 + (lane & 7) * 16;
            int so_ = WS_ST + ((wv * 32 + (lane & 31)) * 2 + (h >> 4)) * 128 + un * 16;
            int dst = (lane & 12) ? OOB : WS_X1 + (h * 4 + cq) * 1024 + wv * 256 + g * 64 + (lane & 3) * 16;
            PIN(ow); PIN(oo); PIN(so_); PIN(orr); PIN(dst); PIN(ww); PIN(wr);
            v4u a[8];
            if (wave >= 4) {
                SPIN4(2);
                slab_frags<4, 4, 8, 32>(L + L_KV + 32 * KVS, lane, wv, a);
            }
#pragma unroll 1
            for (int n = 0; n < 64 * SPIN && flag[20 + wv] == 0; ++n) {}
            until(__builtin_amdgcn_readfirstlane(flag[20 + wv]) + T_O - (h < 16 ? T_LATE : 0));
            v4u o[4], m4 = ld16(rs, so_);
            __builtin_amdgcn_sched_barrier(0);
#pragma unroll
            for (int i = 0; i < 4; ++i) { o[i] = ld16(rs, oo + i * 8192); __builtin_amdgcn_sched_barrier(0); }
            m4 ^= e4;
            if (__any(or4(m4) & 0xffu)) gather_all<1>(rs, so_, 0, 0, e4, 0xffu, *(v4u(*)[1])&m4);
            const float mv = bitsf((h & 1) ? m4.z : m4.x), lv = bitsf((h & 1) ? m4.w : m4.y);
            float mx = mv;
            asm volatile("s_nop 1\n\tv_max_f32_dpp %0, %0, %0 quad_perm:[1,0,3,2] row_mask:0xf bank_mask:0xf\n\ts_nop 1\n\tv_max_f32_dpp %0, %0, %0 quad_perm:[2,3,0,1] row_mask:0xf bank_mask:0xf\n\t"
                         "s_nop 1\n\tv_max_f32_dpp %0, %0, %0 row_ror:4 row_mask:0xf bank_mask:0xf\n\ts_nop 1\n\tv_max_f32_dpp %0, %0, %0 row_ror:8 row_mask:0xf bank_mask:0xf\n\ts_nop 1" : "+v"(mx));
            mx = max16(mx);
            const float wgt = __builtin_amdgcn_exp2f(mv - mx);
            *(LAS float*)ww = wgt;
            FENCE();
            const f32x4 wt = *(LAS f32x4*)wr;
            const float inv = __builtin_amdgcn_rcpf(bitsf(__builtin_amdgcn_readfirstlane(fbits(sum16(col_sum(quad_sum(wgt * lv)))))));
#pragma unroll
            for (int i = 0; i < 4; ++i) o[i] ^= pat;
            if (__any((or4(o[0]) | or4(o[1]) | or4(o[2]) | or4(o[3])) & TM)) {
                gather_masked<4, 1>(rs, oo, 8192, pat, TM, o);
            }
            float acc[8];
#pragma unroll
            for (int k = 0; k < 4; ++k) {
                acc[2 * k] = mlo(o[0][k]) * wt[0] + mlo(o[1][k]) * wt[1] + mlo(o[2][k]) * wt[2] + mlo(o[3][k]) * wt[3];
                acc[2 * k + 1] = mhi(o[0][k]) * wt[0] + mhi(o[1][k]) * wt[1] + mhi(o[2][k]) * wt[2] + mhi(o[3][k]) * wt[3];
            }
            float e0 = rs16(rs32(acc[0], acc[4]), rs32(acc[2], acc[6])), e1 = rs16(rs32(acc[1], acc[5]), rs32(acc[3], acc[7]));
            e0 += dpp<0x128>(e0); e1 += dpp<0x128>(e1);
            const float ov = ((lane & 8) ? e1 : e0) * inv;
            *(LAS u16*)ow = (u16)pk(ov, ov);
            BAR();
            if (wave >= 4) {
                f32x4 d0, d1;
                slab_gemv<4>(a, orr, d0, d1);
                st16(v4u{tagq(d0[0], d0[1], ex.x), tagq(d0[2], d0[3], ex.y), tagq(d1[0], d1[1], ex.z), tagq(d1[2], d1[3], ex.w)}, rs, dst);
            }
        }
        if (r < 8 && wave == 1) {
            const int m0 = w * 8 + ((lane >> 2) & 1) * 4, fb = lane & 3;
            const int xo = ((m0 >> 3) & 7) * 64 + fb * 16 + (m0 & 4) * 2;
            const v2u eh = *(LAS v2u*)(ept + (xo & 127)) & TM;
            const v4u e5 = (*(LAS v4u*)(ept + ((lane & 7) << 4)) >> 8) & 0xffu;
            v2u hp, xr = {0u, 0u};
            if (lane < 8) xr = *(const GAS v2u*)((const GAS u16*)p.x + fb * 512 + m0);
            __builtin_amdgcn_s_sleep(20);
            const unsigned tl = due_time<8>(rs, ept, WS_R2 + lane * 128, 0, ld16(rs, WS_R2 + lane * 128));
            until(tl + T_HD - 70);
#pragma unroll 1
            for (int n = 0; n < SPIN; ++n) {
                FENCE();
                hp = ld8(rs, WS_X2 + ((lane >> 3) * 8 + (m0 >> 6)) * 512 + xo) ^ eh;
                if (!__any((hp.x | hp.y) & TM)) break;
                __builtin_amdgcn_s_sleep(8);
            }
            f32x4 hx = {blo(hp.x) + blo(xr.x), bhi(hp.x) + bhi(xr.x), blo(hp.y) + blo(xr.y), bhi(hp.y) + bhi(xr.y)};
            GAS u16* op = (GAS u16*)p.out + fb * 512 + m0 + g;
            PIN(hx); PIN(op);
            until(tl + T_HD);
            v4u d[6];
#pragma unroll 1
            for (int n = 0; n < SPIN; ++n) {
                FENCE();
#pragma unroll
                for (int i = 0; i < 6; ++i) d[i] = ld16(rs, (i < 5 || lane < 24) ? WS_HD + (w * 48 + i * 8) * 128 + lane * 16 : OOB);
                unsigned bad = 0;
#pragma unroll
                for (int i = 0; i < 6; ++i) { if (i < 5 || lane < 24) d[i] ^= e5; bad |= or4(d[i]); }
                if (!__any(bad & 0xffu)) break;
            }
            const f32x4 s = ((hx + __builtin_bit_cast(f32x4, d[0])) + (__builtin_bit_cast(f32x4, d[1]) + __builtin_bit_cast(f32x4, d[2]))) +
                            ((__builtin_bit_cast(f32x4, d[3]) + __builtin_bit_cast(f32x4, d[4])) + __builtin_bit_cast(f32x4, d[5]));
            float z = rs16(rs32(s[0], s[2]), rs32(s[1], s[3]));
            z += dpp<0x128>(z);
            if (!(lane & 8)) *op = (u16)pk(z, z);
            if (w == 0 && lane < 32) st4(*(LAS unsigned*)(ept + lane * 4), rs, WS_EP + lane * 4);
        }
    } else if (r < 24) {
        const int ms = r - 16;
        VMWAIT(0);
        const int u = i15 >> 2, bb = i15 & 3;
        const int fo = ((lane >> 1) & 3) * 272 + (lane >> 3) * 32 + (lane & 1) * 16;
        const v4u pat = *(LAS v4u*)(ept + (((g & 1) * 4 + bb) << 4)) & 1u;
        const v2u ef = *(LAS v2u*)(ept + i15 * 8);
        v4u a[8];
        blk_frags<2, PP>(L + L_KV + wave * 8 * PP, lane, a);
        LAS unsigned char* rw = L + L_RED + wave * 1088 + bb * 272 + u * 64 + g * 16;
        LAS unsigned char* rr = L + L_RED + fo;
        int dst = WS_X2 + (xg * 8 + ms) * 512 + lane * 8;
        PIN(rw); PIN(rr); PIN(dst);
        v4u c[2];
        gather_all<2>(rs, WS_X1 + ((xg * 4 + (wave >> 1)) * 4 + u) * 1024 + (wave & 1) * 512 + g * 64 + bb * 16, 0, 256, pat, 1u, c);
        f32x4 d[4];
#pragma unroll
        for (int t = 0; t < 4; ++t) d[t] = mfma(a[2 * t], c[0], f32x4{0.f, 0.f, 0.f, 0.f});
#pragma unroll
        for (int t = 0; t < 4; ++t) d[t] = mfma(a[2 * t + 1], c[1], d[t]);
        const f32x4 da = rs8x4<7>(d[0], d[2]), db = rs8x4<7>(d[1], d[3]);
        *(LAS f32x4*)rw = rs4x4(da, db);
        BAR();
        if (wave == 1) ring(rs, ept, WS_R2 + (w - 128) * 128, lane);
        if (wave == 0) {
            f32x4 v[8];
#pragma unroll
            for (int i = 0; i < 8; ++i) v[i] = *(LAS f32x4*)(rr + i * 1088);
            const f32x4 s = ((v[0] + v[1]) + (v[2] + v[3])) + ((v[4] + v[5]) + (v[6] + v[7]));
            st8(v2u{tagb(s[0], s[1], ef.x), tagb(s[2], s[3], ef.y)}, rs, dst);
        }
    } else if (roleE) {
        const int e = ef_;
        const int u = i15 >> 2, bb = i15 & 3;
        const v4u e4 = *(LAS v4u*)(ept + (((g & 1) * 4 + bb) << 4));
        const v4u pat = e4 & TM;
        v4u etag = (e4 >> 8) & 0xffu;
        const v4u xf = *(const GAS v4u*)((const GAS u16*)p.x + bb * 512 + wave * 64 + (u >> 1) * 32 + g * 8);
        if (tid < 16) *(LAS unsigned*)(L + L_ACT + 320 + tid * 4) = 0u;
        VMWAIT(0);
        v4u a[8], dn[4];
        blk_frags<2, PP>(L + L_KV + wave * 8 * PP, lane, a);
        {
            const v4u ones = {0x3f803f80u, 0x3f803f80u, 0x3f803f80u, 0x3f803f80u};
#pragma unroll
            for (int t = 0; t < 4; ++t) {
                f32x4 d = mfma(a[2 * t], ones, f32x4{0.f, 0.f, 0.f, 0.f});
                d = mfma(a[2 * t + 1], ones, d);
                if (i15 == 0) *(LAS f32x4*)(L + L_CS + wave * 256 + t * 64 + g * 16) = d;
            }
        }
        BAR();
        blk_frags<1, PP>(L + L_KV + L_DN + wave * 4 * PP, lane, dn);
        float cs = 0.f;
#pragma unroll
        for (int i = 0; i < 8; ++i) cs += *(LAS float*)(L + L_CS + i * 256 + lane * 4);
        f32x4 xa = unp01(xf), xb = unp23(xf);
        LAS unsigned char* rw = L + L_RED + wave * 1088 + bb * 272 + g * 16;
        LAS unsigned char* sw = L + L_STAT + bb * 64 + wave * 8;
        LAS unsigned char* rr = L + L_RED + wv * 272 + lane * 4;
        LAS unsigned char* sr = L + L_STAT + wv * 64 + (lane & 7) * 8;
        LAS unsigned char* aw = L + L_ACT + wv * 80 + (lane & 31) * 2;
        LAS unsigned char* ar[4];
#pragma unroll
        for (int t = 0; t < 4; ++t) { ar[t] = L + L_ACT + (u == t ? bb * 80 : 320) + g * 16; PIN(ar[t]); PIN(etag[t]); PIN(xa[t]); PIN(xb[t]); }
        int dst = WS_HD + (((wave * 4 + u) * 2 + (g >> 1)) * 48 + e) * 128 + (g & 1) * 64 + bb * 16;
        PIN(rw); PIN(sw); PIN(rr); PIN(sr); PIN(aw); PIN(dst); PIN(cs);
        v4u c[4];
        gather_all<4>(rs, WS_X2 + (u * 8 + wave) * 512 + g * 64 + bb * 16, 4 * 8 * 512, 256, pat, TM, c);
        f32x4 za = rs8x4<1>(unp01(c[0]) + unp01(c[2]), unp01(c[1]) + unp01(c[3])), zb = rs8x4<1>(unp23(c[0]) + unp23(c[2]), unp23(c[1]) + unp23(c[3]));
        add4x8(za, zb);
        za += xa; zb += xb;
        const v4u h0 = {pk(za[0], za[1]), pk(za[2], za[3]), pk(zb[0], zb[1]), pk(zb[2], zb[3])};
        v4u h1;
#pragma unroll
        for (int k = 0; k < 4; ++k) h1[k] = __builtin_amdgcn_update_dpp(0u, h0[k], 0x128, 0xf, 0xf, true);
        f32x4 d[4];
#pragma unroll
        for (int t = 0; t < 4; ++t) d[t] = mfma(a[2 * t], h0, f32x4{0.f, 0.f, 0.f, 0.f});
#pragma unroll
        for (int t = 0; t < 4; ++t) d[t] = mfma(a[2 * t + 1], h1, d[t]);
        float s1 = 0.f, s2 = 0.f;
#pragma unroll
        for (int k = 0; k < 4; ++k) { s1 = dot2(h0[k], 0x3f803f80u, s1); s2 = dot2(h0[k], h0[k], s2); }
        s1 += dpp<0x128>(s1); s2 += dpp<0x128>(s2);
        s1 = sum32(sum16(s1)); s2 = sum32(sum16(s2));
        if (u == 0) {
            *(LAS f32x2*)sw = f32x2{s1, s2};
#pragma unroll
            for (int t = 0; t < 4; ++t) *(LAS f32x4*)(rw + t * 64) = d[t];
        }
        BAR();
        if (wave < 4) {
            f32x2 st = *(LAS f32x2*)sr;
            float v[8];
#pragma unroll
            for (int i = 0; i < 8; ++i) v[i] = *(LAS float*)(rr + i * 1088);
            float t1 = quad_sum(st[0]), t2 = quad_sum(st[1]);
            t1 += dpp<0x124>(t1); t2 += dpp<0x124>(t2);
            const float mean = t1 * (1.f / 512.f);
            const float rstd = __builtin_amdgcn_rsqf(fmaxf(t2 * (1.f / 512.f) - mean * mean, 0.f) + 1e-5f);
            const float z = ((((v[0] + v[1]) + (v[2] + v[3])) + ((v[4] + v[5]) + (v[6] + v[7]))) - mean * cs) * rstd;
            const auto gu = __builtin_amdgcn_permlane32_swap(fbits(z), fbits(z), false, false);
            const float gt = bitsf(gu[0]), up = bitsf(gu[1]);
            const float act = gt * up * __builtin_amdgcn_rcpf(1.f + __builtin_amdgcn_exp2f(gt * -1.4426950408889634f));
            *(LAS u16*)aw = (u16)pk(act, act);
        }
        BAR();
        {
            v4u af[4];
#pragma unroll
            for (int t = 0; t < 4; ++t) af[t] = *(LAS v4u*)ar[t];
            __builtin_amdgcn_sched_barrier(0);
            f32x4 o = {0.f, 0.f, 0.f, 0.f};
#pragma unroll
            for (int t = 0; t < 4; ++t) o = mfma(dn[t], af[t], o);
            st16(((__builtin_bit_cast(v4u, o) + 0x80u) & ~0xffu) | etag, rs, dst);
        }
    }
}
}

extern "C" void kernel_launch(void* const* d_in, const int* in_sizes, int n_in, void* d_out, int out_size, void* d_ws, size_t ws_size,
                              hipStream_t stream) {
    Params p;
    p.x = (const u16*)d_in[0]; p.ckv = (const u16*)d_in[1]; p.wq = (const u16*)d_in[2]; p.wuk = (const u16*)d_in[3];
    p.wuv = (const u16*)d_in[4]; p.wo = (const u16*)d_in[5]; p.wg = (const u16*)d_in[7]; p.wu = (const u16*)d_in[8]; p.wd = (const u16*)d_in[9];
    p.ws = (unsigned char*)d_ws; p.out = (u16*)d_out;
    (void)hipFuncSetAttribute((const void*)fused_decode, hipFuncAttributeMaxDynamicSharedMemorySize, LDS_BYTES);
    hipLaunchKernelGGL(fused_decode, dim3(256), dim3(512), LDS_BYTES, stream, p);
}
```
